```python
import torch

def _softmax(x, axis=-1):
    return torch.softmax(x, dim=axis)
def _gelu(x):
    return torch.nn.functional.gelu(x, approximate="tanh")
def _silu(x):
    return torch.nn.functional.silu(x)
def _sigmoid(x):
    return torch.sigmoid(x)
def _layernorm(x, axis=-1, eps=1e-5):
    m = x.mean(dim=axis, keepdim=True)
    v = x.var(dim=axis, keepdim=True, unbiased=False)
    return (x - m) / torch.sqrt(v + eps)
def _scatter_add(tgt, idx, src, axis=0):
    return tgt.clone().index_add_(axis, idx, src)
def _segment_sum(src, seg, n):
    out = torch.zeros((n,) + src.shape[1:], dtype=src.dtype)
    return out.index_add_(0, seg, src)
def _pad(x, pw):
    flat = tuple(v for pair in reversed(pw) for v in pair)
    return torch.nn.functional.pad(x, flat)

kernel_name = 'concat_heads__sliding_block_cached__sliding_block_cached__decode'

def setup_inputs():

    _w = (lambda a: (lambda t: t.to(torch.bfloat16) if t.is_floating_point() else t)(torch.as_tensor(a)))
    def _A_setup():
        B, Sq, D = 1, 1, 512
        Hq, Hkv, Sk, Dh = 32, 8, 32768, 128
        assert Hq % Hkv == 0, (Hq, Hkv)
        return {
            "x": _w((lambda *s: torch.randn(*s, dtype=torch.bfloat16) * 0.8)(B, Sq, D)),
            "k_cache": _w((lambda *s: torch.randn(*s, dtype=torch.bfloat16) * 0.8)(B, Hkv, Sk, Dh)),
            "v_cache": _w((lambda *s: torch.randn(*s, dtype=torch.bfloat16) * 0.05)(B, Hkv, Sk, Dh)),
            "wq": _w((lambda *s: torch.randn(*s, dtype=torch.bfloat16) * 0.8)(D, Hq * Dh) * (D ** -0.5)),
            "wo": _w((lambda *s: torch.randn(*s, dtype=torch.bfloat16) * 0.05)(Hq * Dh, D) * ((Hq * Dh) ** -0.5)),
        }
    _A_in = _A_setup()

    def _B_setup():
        B, Sq, D = 1, 1, 512
        Hq, Hkv, Sk, Dh = 32, 8, 32768, 128
        assert Hq % Hkv == 0, (Hq, Hkv)
        return {
            "x": _w((lambda *s: torch.randn(*s, dtype=torch.bfloat16) * 0.8)(B, Sq, D)),
            "k_cache": _w((lambda *s: torch.randn(*s, dtype=torch.bfloat16) * 0.8)(B, Hkv, Sk, Dh)),
            "v_cache": _w((lambda *s: torch.randn(*s, dtype=torch.bfloat16) * 0.05)(B, Hkv, Sk, Dh)),
            "wq": _w((lambda *s: torch.randn(*s, dtype=torch.bfloat16) * 0.8)(D, Hq * Dh) * (D ** -0.5)),
            "wo": _w((lambda *s: torch.randn(*s, dtype=torch.bfloat16) * 0.05)(Hq * Dh, D) * ((Hq * Dh) ** -0.5)),
        }
    _B_in = _B_setup()

    return {
        "A_x": _A_in["x"],
        "A_k_cache": _A_in["k_cache"],
        "A_v_cache": _A_in["v_cache"],
        "A_wq": _A_in["wq"],
        "A_wo": _A_in["wo"],
        "B_x": _B_in["x"],
        "B_k_cache": _B_in["k_cache"],
        "B_v_cache": _B_in["v_cache"],
        "B_wq": _B_in["wq"],
        "B_wo": _B_in["wo"],
    }

def reference(A_x, A_k_cache, A_v_cache, A_wq, A_wo, B_x, B_k_cache, B_v_cache, B_wq, B_wo):
    def _A_ref(x, k_cache, v_cache, wq, wo):
        B, Sq, D = x.shape
        Hq, Dh = 32, 128
        Hkv, Sk = k_cache.shape[1], k_cache.shape[2]
        W = min(4096, max(256, Sk // 4))
        rep = Hq // Hkv
        q = (lambda x, *d: x.permute(*d))((x @ wq).reshape(B, Sq, Hq, Dh), 0, 2, 1, 3)
        k = (lambda x, n, axis: torch.repeat_interleave(x, n, dim=axis))(k_cache[:, :, Sk - W:, :], rep, axis=1)
        v = (lambda x, n, axis: torch.repeat_interleave(x, n, dim=axis))(v_cache[:, :, Sk - W:, :], rep, axis=1)
        scores = torch.einsum("bhqd,bhkd->bhqk", q, k) * (Dh ** -0.5)
        p = _softmax(scores, axis=-1)
        o = torch.einsum("bhqk,bhkd->bhqd", p, v)
        return (lambda x, *d: x.permute(*d))(o, 0, 2, 1, 3).reshape(B, Sq, Hq * Dh) @ wo

    def _B_ref(x, k_cache, v_cache, wq, wo):
        B, Sq, D = x.shape
        Hq, Dh = 32, 128
        Hkv, Sk = k_cache.shape[1], k_cache.shape[2]
        W = min(4096, max(256, Sk // 4))
        rep = Hq // Hkv
        q = (lambda x, *d: x.permute(*d))((x @ wq).reshape(B, Sq, Hq, Dh), 0, 2, 1, 3)
        k = (lambda x, n, axis: torch.repeat_interleave(x, n, dim=axis))(k_cache[:, :, Sk - W:, :], rep, axis=1)
        v = (lambda x, n, axis: torch.repeat_interleave(x, n, dim=axis))(v_cache[:, :, Sk - W:, :], rep, axis=1)
        scores = torch.einsum("bhqd,bhkd->bhqk", q, k) * (Dh ** -0.5)
        p = _softmax(scores, axis=-1)
        o = torch.einsum("bhqk,bhkd->bhqd", p, v)
        return (lambda x, *d: x.permute(*d))(o, 0, 2, 1, 3).reshape(B, Sq, Hq * Dh) @ wo

    a = _A_ref(A_x, A_k_cache, A_v_cache, A_wq, A_wo)
    b = _B_ref(B_x, B_k_cache, B_v_cache, B_wq, B_wo)
    return torch.concatenate([a, b], axis=-1)
```

```cpp
#include <hip/hip_runtime.h>
#include <hip/hip_bf16.h>
#include <stdint.h>

#define GAS __attribute__((address_space(1)))
#define LAS __attribute__((address_space(3)))
typedef unsigned v4u __attribute__((ext_vector_type(4)));
typedef unsigned v2u __attribute__((ext_vector_type(2)));
typedef float f32x4 __attribute__((ext_vector_type(4)));
typedef short bf16x8 __attribute__((ext_vector_type(8)));
typedef unsigned long long u64;

#define NT_KV 1
#define NT_WQ 0
#define NT_WO 1
#define STAMPS 0

constexpr int D = 512, HQ = 32, HKV = 8, SK = 32768, DH = 128, W = 4096;
constexpr int NWG = 256, NTHR = 512;
constexpr unsigned SENT = 0xFFFFFFFFu;
constexpr unsigned SPIN_CAP = 1u << 16;
constexpr int AUX_SC1 = 16;

constexpr int WS_EPOCH = 0;
constexpr int WS_QG = 1024;
constexpr int WS_AREA = WS_QG + 16 * 512 * 8;
constexpr int AREA = 2560, AREA_ML = 2048, AREA_IN = 2304;
constexpr int WS_END = WS_AREA + NWG * AREA;
constexpr int WS_STAMP = 1 << 20;

constexpr int L_QP = 0;
constexpr int L_QS = 1024;
constexpr int L_MX = 2048;
constexpr int L_LS = 2176;
constexpr int L_OM = 2304;
constexpr int L_OP = L_OM + 16384;
constexpr int LDS_BYTES = 96 * 1024;

__device__ __forceinline__ float bflo(unsigned u) { return __builtin_bit_cast(float, u << 16); }
__device__ __forceinline__ float bfhi(unsigned u) { return __builtin_bit_cast(float, u & 0xffff0000u); }
__device__ __forceinline__ unsigned cvtpk(float lo, float hi) {
  unsigned r; asm("v_cvt_pk_bf16_f32 %0, %1, %2" : "=v"(r) : "v"(lo), "v"(hi)); return r;
}
template <int CTRL> __device__ __forceinline__ float dpp(float x) {
  return __builtin_bit_cast(float, __builtin_amdgcn_mov_dpp(__builtin_bit_cast(int, x), CTRL, 0xf, 0xf, true));
}
constexpr int XOR1 = 0xB1, XOR2 = 0x4E, XOR7 = 0x141, ROR4 = 0x124, ROR8 = 0x128;
__device__ __forceinline__ float x16_32_max(float x) {
  auto s = __builtin_amdgcn_permlane16_swap(__float_as_uint(x), __float_as_uint(x), false, false);
  x = fmaxf(__uint_as_float(s[0]), __uint_as_float(s[1]));
  auto t = __builtin_amdgcn_permlane32_swap(__float_as_uint(x), __float_as_uint(x), false, false);
  return fmaxf(__uint_as_float(t[0]), __uint_as_float(t[1]));
}
__device__ __forceinline__ float x16_32_sum(float x) {
  auto s = __builtin_amdgcn_permlane16_swap(__float_as_uint(x), __float_as_uint(x), false, false);
  x = __uint_as_float(s[0]) + __uint_as_float(s[1]);
  auto t = __builtin_amdgcn_permlane32_swap(__float_as_uint(x), __float_as_uint(x), false, false);
  return __uint_as_float(t[0]) + __uint_as_float(t[1]);
}
__device__ __forceinline__ f32x4 mfma(v4u a, v4u b, f32x4 c) {
  return __builtin_amdgcn_mfma_f32_16x16x32_bf16(__builtin_bit_cast(bf16x8, a), __builtin_bit_cast(bf16x8, b), c, 0, 0, 0);
}
template <int NT> __device__ __forceinline__ v4u ldg(const GAS unsigned char* p) {
  if constexpr (NT) return __builtin_nontemporal_load((const GAS v4u*)p);
  else return *(const GAS v4u*)p;
}
__device__ __forceinline__ unsigned san(float f) {
  const unsigned u = __float_as_uint(f); return u == SENT ? 0x7FC00000u : u;
}
#define LDSBAR() do { asm volatile("s_waitcnt lgkmcnt(0)" ::: "memory"); __builtin_amdgcn_s_barrier(); asm volatile("" ::: "memory"); } while (0)
#define STAMP(i) do { } while (0)

__global__ void __launch_bounds__(NTHR, 2)
fused_decode(const unsigned char* __restrict__ xA, const unsigned char* __restrict__ kA, const unsigned char* __restrict__ vA,
             const unsigned char* __restrict__ wqA, const unsigned char* __restrict__ woA,
             const unsigned char* __restrict__ xB, const unsigned char* __restrict__ kB, const unsigned char* __restrict__ vB,
             const unsigned char* __restrict__ wqB, const unsigned char* __restrict__ woB,
             unsigned char* __restrict__ out, unsigned char* ws_) {
  extern __shared__ __attribute__((aligned(16))) unsigned char smem[];
  LAS unsigned char* L = (LAS unsigned char*)smem;
  GAS unsigned char* ws = (GAS unsigned char*)ws_;
  const int tid = threadIdx.x, l = tid & 63;
  const int w = __builtin_amdgcn_readfirstlane(tid >> 6);
  const int bid = blockIdx.x, g = bid & 7, j = bid >> 3, b = j >> 4, s = j & 15;
  const int grp = b * 8 + g, me = grp * 16 + s;
  const __amdgpu_buffer_rsrc_t rs = __builtin_amdgcn_make_buffer_rsrc((void*)ws_, 0, WS_END, 0x00020000);
  const GAS unsigned char* xp = (const GAS unsigned char*)(b ? xB : xA);
  const GAS unsigned char* kp = (const GAS unsigned char*)(b ? kB : kA);
  const GAS unsigned char* vp = (const GAS unsigned char*)(b ? vB : vA);
  const GAS unsigned char* wqp = (const GAS unsigned char*)(b ? wqB : wqA);
  const GAS unsigned char* wop = (const GAS unsigned char*)(b ? woB : woA);
  STAMP(0);

  if (tid < AREA / 16) {
    const v4u sv = {SENT, SENT, SENT, SENT};
    __builtin_amdgcn_raw_buffer_store_b128(sv, rs, WS_AREA + me * AREA + tid * 16, 0, AUX_SC1);
  }
  asm volatile("" ::: "memory");
  __builtin_amdgcn_sched_barrier(0);

  const u64 ep = __hip_atomic_load((GAS u64*)(ws + WS_EPOCH + b * 256), __ATOMIC_RELAXED, __HIP_MEMORY_SCOPE_AGENT);
  const v2u xv = *(const GAS v2u*)(xp + (unsigned)(w * 128 + (l >> 2) * 8));
  v4u wq[4];
  {
    const GAS unsigned char* base = wqp + (size_t)(w * 64) * 8192 + (g * 512 + s * 32) * 2;
    const unsigned off = (unsigned)((l >> 2) * 4 * 8192 + (l & 3) * 16);
#pragma unroll
    for (int i = 0; i < 4; ++i) wq[i] = ldg<NT_WQ>(base + i * 8192 + off);
  }
  const size_t kvbase = ((size_t)g * SK + (SK - W) + s * 256 + w * 32) * 256;
  v4u kf[8];
  {
    const GAS unsigned char* base = kp + kvbase;
    const unsigned off = (unsigned)(((l & 15) >> 2) * 256 + (l >> 4) * 64 + (l & 3) * 16);
#pragma unroll
    for (int m = 0; m < 8; ++m) kf[m] = ldg<NT_KV>(base + m * 1024 + off);
  }
  __builtin_amdgcn_sched_barrier(0);
  const u64 tag = ((ep + 1) & 0xFFFFFFFFFFFFull);

  {
    float acc[8];
#pragma unroll
    for (int c = 0; c < 8; ++c) acc[c] = 0.f;
    const float xs[4] = {bflo(xv.x), bfhi(xv.x), bflo(xv.y), bfhi(xv.y)};
#pragma unroll
    for (int i = 0; i < 4; ++i)
#pragma unroll
      for (int c = 0; c < 4; ++c) {
        acc[2 * c] = fmaf(xs[i], bflo(wq[i][c]), acc[2 * c]);
        acc[2 * c + 1] = fmaf(xs[i], bfhi(wq[i][c]), acc[2 * c + 1]);
      }
#pragma unroll
    for (int c = 0; c < 8; ++c) {
      float t = acc[c];
      t += dpp<ROR4>(t); t += dpp<ROR8>(t);
      acc[c] = x16_32_sum(t);
    }
    if (l < 4) {
      LAS f32x4* qp = (LAS f32x4*)(L + L_QP + w * 128 + l * 32);
      qp[0] = f32x4{acc[0], acc[1], acc[2], acc[3]};
      qp[1] = f32x4{acc[4], acc[5], acc[6], acc[7]};
    }
  }
  STAMP(1);
  LDSBAR();
  if (w == 0 && l < 32) {
    float q = 0.f;
#pragma unroll
    for (int ww = 0; ww < 8; ++ww) q += ((LAS float*)(L + L_QP))[ww * 32 + l];
    const unsigned qb = cvtpk(q, q) & 0xffffu;
    __hip_atomic_store((GAS u64*)(ws + WS_QG) + grp * 512 + s * 32 + l, (tag << 16) | qb, __ATOMIC_RELAXED, __HIP_MEMORY_SCOPE_AGENT);
  }
  STAMP(2);
  GAS u64* qg = (GAS u64*)(ws + WS_QG) + grp * 512 + tid;
  u64 qx = __hip_atomic_load(qg, __ATOMIC_RELAXED, __HIP_MEMORY_SCOPE_AGENT);
  const int cb = s & 7, hp = s >> 3, pi = g * 2 + hp;
  GAS u64* tk = (GAS u64*)(ws + WS_QG) + (b * 8 + ((l & 15) >> 1)) * 512 + (((l & 1) * 8 + cb) * 32);
  u64 tx = tag << 16;
  if (w == 0) tx = __hip_atomic_load(tk, __ATOMIC_RELAXED, __HIP_MEMORY_SCOPE_AGENT);
  __builtin_amdgcn_sched_barrier(0);
  v4u vf[8];
  {
    const GAS unsigned char* base = vp + kvbase;
#pragma unroll
    for (int m = 0; m < 8; ++m) vf[m] = ldg<NT_KV>(base + m * 1024 + (unsigned)(l * 16));
  }
  __builtin_amdgcn_sched_barrier(0);
  for (unsigned spins = 0; !__all((qx >> 16) == tag && (tx >> 16) == tag) && spins < SPIN_CAP; ++spins) {
    qx = __hip_atomic_load(qg, __ATOMIC_RELAXED, __HIP_MEMORY_SCOPE_AGENT);
    if (w == 0) tx = __hip_atomic_load(tk, __ATOMIC_RELAXED, __HIP_MEMORY_SCOPE_AGENT);
  }
  ((LAS unsigned short*)(L + L_QS))[tid] = (unsigned short)qx;
  STAMP(3);
  LDSBAR();
  const v4u qf = *(LAS v4u*)(L + L_QS + ((l & 15) >> 2) * 256 + (l >> 4) * 64 + (l & 3) * 16);
  float sc[8];
#pragma unroll
  for (int m = 0; m < 8; ++m) {
    const f32x4 d = mfma(kf[m], qf, f32x4{0.f, 0.f, 0.f, 0.f});
    sc[m] = (dpp<0x00>(d[0]) + dpp<0x55>(d[1])) + (dpp<0xAA>(d[2]) + dpp<0xFF>(d[3]));
  }
  float mx = fmaxf(fmaxf(fmaxf(sc[0], sc[1]), fmaxf(sc[2], sc[3])), fmaxf(fmaxf(sc[4], sc[5]), fmaxf(sc[6], sc[7])));
  mx = x16_32_max(mx);
  if (l < 16 && (l & 3) == 0) ((LAS float*)(L + L_MX))[w * 4 + (l >> 2)] = mx;
  STAMP(4);
  v4u wo[4];
  {
    const GAS unsigned char* base = wop + (size_t)(g * 512 + hp * 256 + w * 32) * 1024 + cb * 128;
    const unsigned off = (unsigned)((l >> 3) * 4 * 1024 + (l & 7) * 16);
#pragma unroll
    for (int i = 0; i < 4; ++i) wo[i] = ldg<NT_WO>(base + i * 1024 + off);
  }
  __builtin_amdgcn_sched_barrier(0);
  LDSBAR();
  constexpr float C2 = 0.088388347648318440f * 1.4426950408889634f;
  float M;
  {
    const int h = (l & 15) >> 2;
    M = ((LAS float*)(L + L_MX))[h];
#pragma unroll
    for (int ww = 1; ww < 8; ++ww) M = fmaxf(M, ((LAS float*)(L + L_MX))[ww * 4 + h]);
  }
  const float Mc = M * C2;
  v4u pf;
  {
    float p[8];
#pragma unroll
    for (int m = 0; m < 8; ++m) p[m] = __builtin_amdgcn_exp2f(fmaf(sc[m], C2, -Mc));
    pf = v4u{cvtpk(p[0], p[1]), cvtpk(p[2], p[3]), cvtpk(p[4], p[5]), cvtpk(p[6], p[7])};
  }
  const v4u ones = {0x3F803F80u, 0x3F803F80u, 0x3F803F80u, 0x3F803F80u};
  const f32x4 lsum = mfma(pf, ones, f32x4{0.f, 0.f, 0.f, 0.f});
  if ((l & 15) == 0) ((LAS float*)(L + L_LS))[w * 4 + (l >> 4)] = lsum[0];
  STAMP(5);
  {
    float o[8];
#pragma unroll
    for (int d = 0; d < 8; ++d) {
      const unsigned sel = (d & 1) ? 0x07060302u : 0x05040100u;
      v4u bfr;
#pragma unroll
      for (int u = 0; u < 4; ++u) bfr[u] = __builtin_amdgcn_perm(vf[2 * u + 1][d >> 1], vf[2 * u][d >> 1], sel);
      const f32x4 dd = mfma(pf, bfr, f32x4{0.f, 0.f, 0.f, 0.f});
      o[d] = dd[0];
    }
    LAS f32x4* om = (LAS f32x4*)(L + L_OM + w * 2048 + l * 16);
    om[0] = f32x4{o[0], o[1], o[2], o[3]};
    om[64] = f32x4{o[4], o[5], o[6], o[7]};
  }
  STAMP(6);
  LDSBAR();
  if (tid < 128) {
    const int lo = (tid >> 5) * 16 + ((tid & 31) >> 1), h2 = tid & 1;
    f32x4 a = {0.f, 0.f, 0.f, 0.f};
#pragma unroll
    for (int ww = 0; ww < 8; ++ww) a += *(LAS f32x4*)(L + L_OM + ww * 2048 + h2 * 1024 + lo * 16);
    const v4u o4 = {san(a[0]), san(a[1]), san(a[2]), san(a[3])};
    __builtin_amdgcn_raw_buffer_store_b128(o4, rs, WS_AREA + me * AREA + tid * 16, 0, AUX_SC1);
  } else if (w == 2) {
    if (l < 4) {
      float Mh = ((LAS float*)(L + L_MX))[l], Lh = 0.f;
#pragma unroll
      for (int ww = 1; ww < 8; ++ww) Mh = fmaxf(Mh, ((LAS float*)(L + L_MX))[ww * 4 + l]);
#pragma unroll
      for (int ww = 0; ww < 8; ++ww) Lh += ((LAS float*)(L + L_LS))[ww * 4 + l];
      const v2u ml = {san(Mh * C2), san(Lh)};
      __builtin_amdgcn_raw_buffer_store_b64(ml, rs, WS_AREA + me * AREA + AREA_ML + l * 8, 0, AUX_SC1);
    }
  }
  STAMP(7);
  float ov[4];
  {
    const int head = hp * 2 + (w >> 2), rr = l >> 3, cc = l & 7;
    const int a0 = WS_AREA + (grp * 16 + cc) * AREA;
    const int oo = head * 512 + (w & 3) * 128 + rr * 16;
    v4u o0, o1; v2u m0, m1;
    for (unsigned spins = 0;; ++spins) {
      asm volatile("" ::: "memory");
      o0 = __builtin_amdgcn_raw_buffer_load_b128(rs, a0 + oo, 0, AUX_SC1);
      o1 = __builtin_amdgcn_raw_buffer_load_b128(rs, a0 + 8 * AREA + oo, 0, AUX_SC1);
      m0 = __builtin_amdgcn_raw_buffer_load_b64(rs, a0 + AREA_ML + head * 8, 0, AUX_SC1);
      m1 = __builtin_amdgcn_raw_buffer_load_b64(rs, a0 + 8 * AREA + AREA_ML + head * 8, 0, AUX_SC1);
      const bool ok = o0[0] != SENT && o0[1] != SENT && o0[2] != SENT && o0[3] != SENT &&
                      o1[0] != SENT && o1[1] != SENT && o1[2] != SENT && o1[3] != SENT &&
                      m0[0] != SENT && m0[1] != SENT && m1[0] != SENT && m1[1] != SENT;
      if (__all(ok) || spins >= SPIN_CAP) break;
    }
    const float ma = __uint_as_float(m0[0]), mb = __uint_as_float(m1[0]);
    float Mg = fmaxf(ma, mb);
    Mg = fmaxf(Mg, dpp<XOR1>(Mg)); Mg = fmaxf(Mg, dpp<XOR2>(Mg)); Mg = fmaxf(Mg, dpp<XOR7>(Mg));
    const float wa = __builtin_amdgcn_exp2f(ma - Mg), wb = __builtin_amdgcn_exp2f(mb - Mg);
    float Lg = fmaf(wa, __uint_as_float(m0[1]), wb * __uint_as_float(m1[1]));
    Lg += dpp<XOR1>(Lg); Lg += dpp<XOR2>(Lg); Lg += dpp<XOR7>(Lg);
    const float inv = 1.0f / Lg;
#pragma unroll
    for (int i = 0; i < 4; ++i) {
      float t = fmaf(wa, __uint_as_float(o0[i]), wb * __uint_as_float(o1[i]));
      t += dpp<XOR1>(t); t += dpp<XOR2>(t); t += dpp<XOR7>(t);
      ov[i] = t * inv;
    }
  }
  STAMP(8);
  {
    float acc[8];
#pragma unroll
    for (int c = 0; c < 8; ++c) acc[c] = 0.f;
#pragma unroll
    for (int i = 0; i < 4; ++i)
#pragma unroll
      for (int c = 0; c < 4; ++c) {
        acc[2 * c] = fmaf(ov[i], bflo(wo[i][c]), acc[2 * c]);
        acc[2 * c + 1] = fmaf(ov[i], bfhi(wo[i][c]), acc[2 * c + 1]);
      }
#pragma unroll
    for (int c = 0; c < 8; ++c) {
      float t = acc[c];
      t += dpp<ROR8>(t);
      acc[c] = x16_32_sum(t);
    }
    if (l < 8) {
      LAS f32x4* op = (LAS f32x4*)(L + L_OP + w * 256 + l * 32);
      op[0] = f32x4{acc[0], acc[1], acc[2], acc[3]};
      op[1] = f32x4{acc[4], acc[5], acc[6], acc[7]};
    }
  }
  STAMP(9);
  LDSBAR();
  if (w == 0) {
    float t = 0.f;
#pragma unroll
    for (int ww = 0; ww < 8; ++ww) t += ((LAS float*)(L + L_OP))[ww * 64 + l];
    const int cons = (b * 8 + (l >> 3)) * 16 + ((l >> 2) & 1) * 8 + cb;
    __builtin_amdgcn_raw_buffer_store_b32(san(t), rs, WS_AREA + cons * AREA + AREA_IN + pi * 16 + (l & 3) * 4, 0, AUX_SC1);
    STAMP(10);
    unsigned v;
    for (unsigned spins = 0;; ++spins) {
      asm volatile("" ::: "memory");
      v = __builtin_amdgcn_raw_buffer_load_b32(rs, WS_AREA + me * AREA + AREA_IN + l * 4, 0, AUX_SC1);
      if (__all(v != SENT) || spins >= SPIN_CAP) break;
    }
    float r = __uint_as_float(v);
    r += dpp<ROR4>(r); r += dpp<ROR8>(r);
    r = x16_32_sum(r);
    const float r1 = dpp<0x55>(r), r2 = dpp<0xAA>(r), r3 = dpp<0xFF>(r);
    if (l == 0) {
      const v2u o2 = {cvtpk(r, r1), cvtpk(r2, r3)};
      *(GAS v2u*)((GAS unsigned char*)out + (b * 512 + cb * 64 + pi * 4) * 2) = o2;
      if (g == 0 && s == 0)
        __hip_atomic_store((GAS u64*)(ws + WS_EPOCH + b * 256), tag, __ATOMIC_RELAXED, __HIP_MEMORY_SCOPE_AGENT);
    }
    STAMP(11);
  }
}

extern "C" void kernel_launch(void* const* d_in, const int* in_sizes, int n_in,
                              void* d_out, int out_size, void* d_ws, size_t ws_size,
                              hipStream_t stream) {
  (void)in_sizes; (void)n_in; (void)out_size; (void)ws_size;
  (void)hipFuncSetAttribute((const void*)fused_decode, hipFuncAttributeMaxDynamicSharedMemorySize, LDS_BYTES);
  typedef const unsigned char* P;
  hipLaunchKernelGGL(fused_decode, dim3(NWG), dim3(NTHR), LDS_BYTES, stream,
                     (P)d_in[0], (P)d_in[1], (P)d_in[2], (P)d_in[3], (P)d_in[4],
                     (P)d_in[5], (P)d_in[6], (P)d_in[7], (P)d_in[8], (P)d_in[9],
                     (unsigned char*)d_out, (unsigned char*)d_ws);
}
```

```cpp
#include <hip/hip_runtime.h>
#include <hip/hip_bf16.h>
#include <stdint.h>

#define GAS __attribute__((address_space(1)))
#define LAS __attribute__((address_space(3)))
typedef unsigned v4u __attribute__((ext_vector_type(4)));
typedef unsigned v2u __attribute__((ext_vector_type(2)));
typedef float f32x4 __attribute__((ext_vector_type(4)));
typedef short bf16x8 __attribute__((ext_vector_type(8)));
typedef unsigned long long u64;

#define STAMPS 0
#define WQPAT 2
#define KDELAY 4
#define WIN 8
#define NOPOLL 0
#define NORESET 0
#define WOEARLY 2
#define POS1 6
#define POS2 1

constexpr int HKV = 8, SK = 32768, W = 4096;
constexpr int NWG = 256, NPW = 4, NTHR = (NPW + 8) * 64;
constexpr unsigned SENT = 0xFFFFFFFFu;
constexpr unsigned SPIN_CAP = 1u << 16;
constexpr int AUX_SC1 = 16, AUX_NT = 2;

constexpr int WS_EPOCH = 0;
constexpr int WS_QG = 1024;
constexpr int WS_OR = WS_QG + 16 * 256 * 16;
constexpr int WS_IN = WS_OR + NWG * 2048;
constexpr int WS_END = WS_IN + 32 * 16384;
constexpr int WS_STAMP = 2 << 20;
static_assert(WS_OR % 128 == 0 && WS_IN % 128 == 0 && WS_END <= WS_STAMP, "line-aligned records");

constexpr int L_QP = 0;
constexpr int L_QS = 1024;
constexpr int L_MX = 2048;
constexpr int L_LS = 2176;
constexpr int L_CNT = 2304, L_TOK = 2308, L_SIG = 2312, L_TAG = 2320;
constexpr int L_OV = 2432;
constexpr int L_FIN = 2560;
constexpr int L_OM = 4096;
constexpr int LDS_BYTES = 96 * 1024;

__device__ __forceinline__ float bflo(unsigned u) { return __builtin_bit_cast(float, u << 16); }
__device__ __forceinline__ float bfhi(unsigned u) { return __builtin_bit_cast(float, u & 0xffff0000u); }
typedef __bf16 bf16x2 __attribute__((ext_vector_type(2)));
typedef float f32x2 __attribute__((ext_vector_type(2)));
__device__ __forceinline__ unsigned cvtpk(float lo, float hi) {
  const f32x2 v = {lo, hi};
  return __builtin_bit_cast(unsigned, __builtin_convertvector(v, bf16x2));
}
template <int CTRL> __device__ __forceinline__ float dpp(float x) {
  return __builtin_bit_cast(float, __builtin_amdgcn_mov_dpp(__builtin_bit_cast(int, x), CTRL, 0xf, 0xf, true));
}
constexpr int XOR1 = 0xB1, XOR2 = 0x4E, XOR7 = 0x141, ROR4 = 0x124, ROR8 = 0x128;
__device__ __forceinline__ float x16_32_max(float x) {
  auto s = __builtin_amdgcn_permlane16_swap(__float_as_uint(x), __float_as_uint(x), false, false);
  x = fmaxf(__uint_as_float(s[0]), __uint_as_float(s[1]));
  auto t = __builtin_amdgcn_permlane32_swap(__float_as_uint(x), __float_as_uint(x), false, false);
  return fmaxf(__uint_as_float(t[0]), __uint_as_float(t[1]));
}
__device__ __forceinline__ float x16_32_sum(float x) {
  auto s = __builtin_amdgcn_permlane16_swap(__float_as_uint(x), __float_as_uint(x), false, false);
  x = __uint_as_float(s[0]) + __uint_as_float(s[1]);
  auto t = __builtin_amdgcn_permlane32_swap(__float_as_uint(x), __float_as_uint(x), false, false);
  return __uint_as_float(t[0]) + __uint_as_float(t[1]);
}
__device__ __forceinline__ f32x4 mfma(v4u a, v4u b, f32x4 c) {
  return __builtin_amdgcn_mfma_f32_16x16x32_bf16(__builtin_bit_cast(bf16x8, a), __builtin_bit_cast(bf16x8, b), c, 0, 0, 0);
}
__device__ __forceinline__ unsigned san(float f) {
  const unsigned u = __float_as_uint(f); return u == SENT ? 0x7FC00000u : u;
}
#define LDSBAR() do { asm volatile("s_waitcnt lgkmcnt(0)" ::: "memory"); __builtin_amdgcn_s_barrier(); asm volatile("" ::: "memory"); } while (0)
#define SB() __builtin_amdgcn_sched_barrier(0)
#define STAMP(i) do { } while (0)
#define STAMP_FLUSH() do { } while (0)
__device__ __forceinline__ __amdgpu_buffer_rsrc_t mkrs(const void* p, int bytes) { return __builtin_amdgcn_make_buffer_rsrc((void*)p, 0, bytes, 0x00020000); }
template <int AUX> __device__ __forceinline__ v4u bld(__amdgpu_buffer_rsrc_t r, int voff, int soff) { return __builtin_amdgcn_raw_buffer_load_b128(r, voff, soff, AUX); }
__global__ void __launch_bounds__(NTHR, 3)
fused_decode(const unsigned char* __restrict__ xA, const unsigned char* __restrict__ kA, const unsigned char* __restrict__ vA,
             const unsigned char* __restrict__ wqA, const unsigned char* __restrict__ woA,
             const unsigned char* __restrict__ xB, const unsigned char* __restrict__ kB, const unsigned char* __restrict__ vB,
             const unsigned char* __restrict__ wqB, const unsigned char* __restrict__ woB,
             unsigned char* __restrict__ out, unsigned char* ws_) {
  extern __shared__ __attribute__((aligned(16))) unsigned char smem[];
  LAS unsigned char* L = (LAS unsigned char*)smem;
  GAS unsigned char* ws = (GAS unsigned char*)ws_;
  const int tid = threadIdx.x, l = tid & 63;
  const int wv = __builtin_amdgcn_readfirstlane(tid >> 6);
  const int bid = blockIdx.x, g = bid & 7, j = bid >> 3, b = j >> 4, s = j & 15;
  const int grp = b * 8 + g, me = grp * 16 + s;
  const bool cons = (s & 7) == 0;
  const int cidx = b * 16 + g * 2 + (s >> 3);
  const __amdgpu_buffer_rsrc_t rs = mkrs(ws_, WS_END);
  STAMP(0);

  if (wv < NPW) {
    __builtin_amdgcn_s_setprio(3);
    const int p = wv;
    if (tid == 0) *(LAS v4u*)(L + L_CNT) = v4u{0u, 0u, 0u, 0u};
    SB();
    const unsigned ep = __hip_atomic_load((GAS unsigned*)(ws + WS_EPOCH + b * 256), __ATOMIC_RELAXED, __HIP_MEMORY_SCOPE_AGENT);
    const __amdgpu_buffer_rsrc_t rx = mkrs(b ? xB : xA, 1024);
    const __amdgpu_buffer_rsrc_t rq = mkrs(b ? wqB : wqA, 512 * 8192);
    v4u xv, wq[8];
    if constexpr (WQPAT == 0) {
      xv = bld<0>(rx, (l >> 2) * 16, p * 256);
      const int vo = (l >> 2) * 8 * 8192 + (l & 3) * 16, so = p * 128 * 8192 + (g * 512 + s * 32) * 2;
#pragma unroll
      for (int i = 0; i < 8; ++i) wq[i] = bld<AUX_NT>(rq, vo, so + i * 8192);
    } else {
      xv = bld<0>(rx, (l >> 3) * 16, (s & 1) * 512 + p * 128);
      const int vo = (l >> 3) * 8 * 8192 + (l & 7) * 16, so = ((s & 1) * 256 + p * 64) * 8192 + (g * 512 + (s >> 1) * 64) * 2;
#pragma unroll
      for (int i = 0; i < 8; ++i) wq[i] = bld<AUX_NT>(rq, vo, so + i * 8192);
    }
    SB();
    if (!NORESET) {
      const v4u sv = {SENT, SENT, SENT, SENT};
      constexpr int OOB = 0x7FFF0000;
      __builtin_amdgcn_raw_buffer_store_b128(sv, rs, tid < 128 ? WS_OR + me * 2048 + tid * 16 : OOB, 0, AUX_SC1);
      const int ib = cons ? WS_IN + cidx * 16384 + tid * 16 : OOB;
#pragma unroll
      for (int i = 0; i < 4; ++i) __builtin_amdgcn_raw_buffer_store_b128(sv, rs, ib, i * 4096, AUX_SC1);
    }
    SB();
    if (KDELAY == 0) { LDSBAR(); SB(); }
    float acc[8];
#pragma unroll
    for (int c = 0; c < 8; ++c) acc[c] = 0.f;
#pragma unroll
    for (int i = 0; i < 8; ++i) {
      const float xs = (i & 1) ? bfhi(xv[i >> 1]) : bflo(xv[i >> 1]);
#pragma unroll
      for (int c = 0; c < 4; ++c) {
        acc[2 * c] = fmaf(xs, bflo(wq[i][c]), acc[2 * c]);
        acc[2 * c + 1] = fmaf(xs, bfhi(wq[i][c]), acc[2 * c + 1]);
      }
      if (KDELAY == i + 1) { asm volatile("" :: "v"(acc[0]), "v"(acc[7])); SB(); LDSBAR(); SB(); }
    }
    STAMP(1);
    const unsigned tag = ep + 1u;
#pragma unroll
    for (int c = 0; c < 8; ++c) {
      float t = acc[c];
      if (WQPAT == 0) t += dpp<ROR4>(t);
      t += dpp<ROR8>(t);
      acc[c] = x16_32_sum(t);
    }
    if (l < (WQPAT == 0 ? 4 : 8)) {
      LAS f32x4* qp = (LAS f32x4*)(L + L_QP + p * 256 + l * 32);
      qp[0] = f32x4{acc[0], acc[1], acc[2], acc[3]};
      qp[1] = f32x4{acc[4], acc[5], acc[6], acc[7]};
    }
    asm volatile("s_waitcnt vmcnt(0) lgkmcnt(0)" ::: "memory");
    unsigned arr = 0;
    if (l == 0) arr = __hip_atomic_fetch_add((LAS unsigned*)(L + L_CNT), 1u, __ATOMIC_RELAXED, __HIP_MEMORY_SCOPE_WORKGROUP);
    arr = __builtin_amdgcn_readfirstlane(arr);
    STAMP(2);
    if (arr == NPW - 1) {
      asm volatile("" ::: "memory");
      float q = 0.f;
#pragma unroll
      for (int ww = 0; ww < NPW; ++ww) q += ((volatile LAS float*)(L + L_QP))[ww * 64 + l];
      const unsigned pr = cvtpk(q, dpp<XOR1>(q));
      const u64 gr = ((u64)tag << 32) | pr;
      if (WQPAT == 0) { if (l < 32 && !(l & 1)) __hip_atomic_store((GAS u64*)(ws + WS_QG) + grp * 256 + s * 16 + (l >> 1), gr, __ATOMIC_RELAXED, __HIP_MEMORY_SCOPE_AGENT); }
      else if (!(l & 1)) __hip_atomic_store((GAS u64*)(ws + WS_QG) + (grp * 16 + s) * 32 + (l >> 1), gr, __ATOMIC_RELAXED, __HIP_MEMORY_SCOPE_AGENT);
    }
    STAMP(3);
    if (p == 0 && !NOPOLL) {
      constexpr int NI = WQPAT == 0 ? 2 : 4;
      const int qo = WS_QG + grp * (NI * 1024) + l * 16;
      v4u qa[NI], qb[NI], r[NI];
      auto sig = [&](unsigned m) {
        for (unsigned spins = 0; !(__hip_atomic_load((LAS unsigned*)(L + L_SIG), __ATOMIC_RELAXED, __HIP_MEMORY_SCOPE_WORKGROUP) & m) && spins < (SPIN_CAP << 4); ++spins) __builtin_amdgcn_s_sleep(1);
      };
      sig(1u);
      asm volatile("" ::: "memory");
#pragma unroll
      for (int i = 0; i < NI; ++i) qa[i] = __builtin_amdgcn_raw_buffer_load_b128(rs, qo + i * 1024, 0, AUX_SC1);
      SB();
      STAMP(7);
      sig(2u);
      asm volatile("" ::: "memory");
#pragma unroll
      for (int i = 0; i < NI; ++i) qb[i] = __builtin_amdgcn_raw_buffer_load_b128(rs, qo + i * 1024, 0, AUX_SC1);
      SB();
      STAMP(8);
      bool ok = true;
#pragma unroll
      for (int i = 0; i < NI; ++i) { r[i] = qa[i]; ok = ok && qa[i][1] == tag && qa[i][3] == tag; }
      bool done = __all(ok);
      STAMP(9);
      if (!done) {
        ok = true;
#pragma unroll
        for (int i = 0; i < NI; ++i) { r[i] = qb[i]; ok = ok && qb[i][1] == tag && qb[i][3] == tag; }
        done = __all(ok);
        STAMP(10);
        for (unsigned spins = 0; !done && spins < SPIN_CAP; ++spins) {
          asm volatile("" ::: "memory");
          ok = true;
#pragma unroll
          for (int i = 0; i < NI; ++i) { r[i] = __builtin_amdgcn_raw_buffer_load_b128(rs, qo + i * 1024, 0, AUX_SC1); }
#pragma unroll
          for (int i = 0; i < NI; ++i) ok = ok && r[i][1] == tag && r[i][3] == tag;
          done = __all(ok);
          STAMP(11);
        }
      }
      if (WQPAT == 0) {
#pragma unroll
        for (int i = 0; i < NI; ++i) *(LAS v2u*)(L + L_QS + i * 512 + l * 8) = v2u{r[i][0], r[i][2]};
      } else {
#pragma unroll
        for (int i = 0; i < NI; ++i) {
          float f[4] = {bflo(r[i][0]), bfhi(r[i][0]), bflo(r[i][2]), bfhi(r[i][2])};
#pragma unroll
          for (int c = 0; c < 4; ++c) {
            auto t = __builtin_amdgcn_permlane16_swap(__float_as_uint(f[c]), __float_as_uint(f[c]), false, false);
            f[c] = __uint_as_float(t[0]) + __uint_as_float(t[1]);
          }
          if (!(l & 16)) *(LAS v2u*)(L + L_QS + (2 * i + (l >> 5)) * 128 + (l & 15) * 8) = v2u{cvtpk(f[0], f[1]), cvtpk(f[2], f[3])};
        }
      }
      if (l == 0) *(LAS unsigned*)(L + L_TAG) = tag;
    }
    if (p == 0) STAMP(4);
    LDSBAR();
    STAMP(5);
    if (p == 0) {
      GAS u64* tk = (GAS u64*)(ws + WS_QG) + (WQPAT == 0 ? (b * 8 + ((l & 15) >> 1)) * 256 + (l & 1) * 128 : (b * 8 + ((l & 15) >> 1)) * 512 + (l & 1) * 256);
      for (unsigned spins = 0;; ++spins) {
        const u64 tx = __hip_atomic_load(tk, __ATOMIC_RELAXED, __HIP_MEMORY_SCOPE_AGENT);
        if (__all((unsigned)(tx >> 32) == tag) || spins >= SPIN_CAP) break;
      }
      if (l == 0) __hip_atomic_store((LAS unsigned*)(L + L_TOK), 1u, __ATOMIC_RELAXED, __HIP_MEMORY_SCOPE_WORKGROUP);
      STAMP(6);
    }
    STAMP_FLUSH();
    return;
  }

  const int w = wv - NPW;
  const __amdgpu_buffer_rsrc_t rk = mkrs(b ? kB : kA, HKV * SK * 256);
  const __amdgpu_buffer_rsrc_t rv = mkrs(b ? vB : vA, HKV * SK * 256);
  const __amdgpu_buffer_rsrc_t ro = mkrs(b ? woB : woA, 4096 * 1024);
  const int kvbase = (g * SK + (SK - W) + s * 256 + w * 32) * 256;
  __builtin_amdgcn_s_setprio(2);
  v4u kf[8];
  const int kvo = ((l & 15) >> 2) * 256 + (l >> 4) * 64 + (l & 3) * 16;
  LDSBAR();
  SB();
  v4u vf[8];
#pragma unroll
  for (int i = 0; i < 16; ++i) {
    if (i >= WIN && i < 8 + WIN) { asm volatile("" :: "v"(kf[i - WIN])); SB(); if (i == WIN) STAMP(12); if (i == 7 + WIN) STAMP(13); }
    if (i == 8) __builtin_amdgcn_s_setprio(1);
    if (i < 8) kf[i] = bld<AUX_NT>(rk, kvo + (i & 3) * 1024, kvbase + (i >> 2) * 4096);
    else vf[i - 8] = bld<AUX_NT>(rv, l * 16 + (i & 3) * 1024, kvbase + ((i - 8) >> 2) * 4096);
    SB();
    if (i == 7 + POS1 || i == 7 + POS2) {
      if (l == 0 && ((i == 7 + POS1 && w == 0) || (i == 7 + POS2 && w == 4)))
        __hip_atomic_fetch_or((LAS unsigned*)(L + L_SIG), w == 0 ? 1u : 2u, __ATOMIC_RELAXED, __HIP_MEMORY_SCOPE_WORKGROUP);
      SB();
    }
  }
  v4u wo[4];
#define WO_LOADS() do { __builtin_amdgcn_s_setprio(0); \
    const int vo = (l >> 3) * 4 * 1024 + (l & 7) * 16, so = (g * 512 + s * 32) * 1024 + w * 128; \
    _Pragma("unroll") for (int i = 0; i < 4; ++i) wo[i] = bld<AUX_NT>(ro, vo + i * 1024, so); \
    SB(); } while (0)
  if (WOEARLY == 1) { WO_LOADS(); }
  STAMP(1);
  LDSBAR();
  STAMP(2);
  const v4u qf = *(LAS v4u*)(L + L_QS + ((l & 15) >> 2) * 256 + (l >> 4) * 64 + (l & 3) * 16);
  float sc[8];
#pragma unroll
  for (int m = 0; m < 8; ++m) {
    const f32x4 d = mfma(kf[m], qf, f32x4{0.f, 0.f, 0.f, 0.f});
    sc[m] = (dpp<0x00>(d[0]) + dpp<0x55>(d[1])) + (dpp<0xAA>(d[2]) + dpp<0xFF>(d[3]));
  }
  float mx = fmaxf(fmaxf(fmaxf(sc[0], sc[1]), fmaxf(sc[2], sc[3])), fmaxf(fmaxf(sc[4], sc[5]), fmaxf(sc[6], sc[7])));
  constexpr float C2 = 0.088388347648318440f * 1.4426950408889634f;
  const float Mc = x16_32_max(mx) * C2;
  if (l < 16 && (l & 3) == 0) ((LAS float*)(L + L_MX))[(l >> 2) * 8 + w] = Mc;
  STAMP(3);
  v4u pf;
  {
    float p[8];
#pragma unroll
    for (int m = 0; m < 8; ++m) p[m] = __builtin_amdgcn_exp2f(fmaf(sc[m], C2, -Mc));
    pf = v4u{cvtpk(p[0], p[1]), cvtpk(p[2], p[3]), cvtpk(p[4], p[5]), cvtpk(p[6], p[7])};
  }
  const v4u ones = {0x3F803F80u, 0x3F803F80u, 0x3F803F80u, 0x3F803F80u};
  const f32x4 lsum = mfma(pf, ones, f32x4{0.f, 0.f, 0.f, 0.f});
  if ((l & 15) == 0) ((LAS float*)(L + L_LS))[(l >> 4) * 8 + w] = lsum[0];
  SB();
  if (WOEARLY == 0) { WO_LOADS(); }
  STAMP(16);
  if (WOEARLY == 2) { SB(); asm volatile("" :: "v"(vf[7])); SB(); STAMP(17); WO_LOADS(); }
  STAMP(4);
  {
    float o[8];
#pragma unroll
    for (int d = 0; d < 8; ++d) {
      const unsigned sel = (d & 1) ? 0x07060302u : 0x05040100u;
      v4u bfr;
#pragma unroll
      for (int u = 0; u < 4; ++u) bfr[u] = __builtin_amdgcn_perm(vf[2 * u + 1][d >> 1], vf[2 * u][d >> 1], sel);
      const f32x4 dd = mfma(pf, bfr, f32x4{0.f, 0.f, 0.f, 0.f});
      o[d] = dd[0];
    }
    LAS f32x4* om = (LAS f32x4*)(L + L_OM + w * 2048 + l * 16);
    om[0] = f32x4{o[0], o[1], o[2], o[3]};
    om[64] = f32x4{o[4], o[5], o[6], o[7]};
  }
  STAMP(5);
  LDSBAR();
  if (w < 2) {
    const int t = w * 64 + l;
    const int lo = (t >> 5) * 16 + ((t & 31) >> 1), h2 = t & 1;
    const f32x4 m0 = *(LAS f32x4*)(L + L_MX + (t >> 5) * 32), m1 = *(LAS f32x4*)(L + L_MX + (t >> 5) * 32 + 16);
    const f32x4 l0 = *(LAS f32x4*)(L + L_LS + (t >> 5) * 32), l1 = *(LAS f32x4*)(L + L_LS + (t >> 5) * 32 + 16);
    f32x4 ow[8];
#pragma unroll
    for (int ww = 0; ww < 8; ++ww) ow[ww] = *(LAS f32x4*)(L + L_OM + ww * 2048 + h2 * 1024 + lo * 16);
    const float Mh = fmaxf(fmaxf(fmaxf(m0[0], m0[1]), fmaxf(m0[2], m0[3])), fmaxf(fmaxf(m1[0], m1[1]), fmaxf(m1[2], m1[3])));
    float e[8], Lh = 0.f;
#pragma unroll
    for (int ww = 0; ww < 8; ++ww) { e[ww] = __builtin_amdgcn_exp2f((ww < 4 ? m0[ww & 3] : m1[ww & 3]) - Mh); Lh = fmaf(e[ww], ww < 4 ? l0[ww & 3] : l1[ww & 3], Lh); }
    f32x4 a = ow[0] * e[0];
#pragma unroll
    for (int ww = 1; ww < 8; ++ww) a += ow[ww] * e[ww];
    const float pinv = __builtin_amdgcn_rcpf(Lh);
    const unsigned plse = __float_as_uint(Mh + __builtin_amdgcn_logf(Lh)) >> (l & 7);
    v4u o4;
#pragma unroll
    for (int i = 0; i < 4; ++i) {
      unsigned u = __float_as_uint(a[i] * pinv);
      u = (u | 1u) == SENT ? 0x7FC00000u : u;
      o4[i] = (u & ~1u) | ((plse >> (8 * i)) & 1u);
    }
    __builtin_amdgcn_raw_buffer_store_b128(o4, rs, WS_OR + me * 2048 + t * 16, 0, AUX_SC1);
  } else if (w == 3) {
    const int pp = l >> 3, a0 = WS_OR + (grp * 16 + pp) * 2048 + s * 128 + (l & 7) * 16;
    v4u o0, o1;
    for (unsigned spins = 0;; ++spins) {
      asm volatile("" ::: "memory");
      o0 = __builtin_amdgcn_raw_buffer_load_b128(rs, a0, 0, AUX_SC1);
      o1 = __builtin_amdgcn_raw_buffer_load_b128(rs, a0 + 8 * 2048, 0, AUX_SC1);
      const bool ok = o0[0] != SENT && o0[1] != SENT && o0[2] != SENT && o0[3] != SENT &&
                      o1[0] != SENT && o1[1] != SENT && o1[2] != SENT && o1[3] != SENT;
      if (__all(ok) || spins >= SPIN_CAP) break;
    }
    STAMP(7);
    unsigned ea = 0, eb = 0;
#pragma unroll
    for (int i = 0; i < 4; ++i) {
      const u64 ba = __ballot(o0[i] & 1u), bb = __ballot(o1[i] & 1u);
      ea |= ((unsigned)(ba >> (l & 56)) & 0xffu) << (8 * i);
      eb |= ((unsigned)(bb >> (l & 56)) & 0xffu) << (8 * i);
    }
    const float ma = __uint_as_float(ea), mb = __uint_as_float(eb);
    float Mg = fmaxf(ma, mb);
    Mg = fmaxf(Mg, dpp<ROR8>(Mg)); Mg = x16_32_max(Mg);
    const float wa = __builtin_amdgcn_exp2f(ma - Mg), wb = __builtin_amdgcn_exp2f(mb - Mg);
    float Lg = wa + wb;
    Lg += dpp<ROR8>(Lg); Lg = x16_32_sum(Lg);
    const float inv = __builtin_amdgcn_rcpf(Lg);
    f32x4 ov;
#pragma unroll
    for (int i = 0; i < 4; ++i) {
      float t = fmaf(wa, __uint_as_float(o0[i] & ~1u), wb * __uint_as_float(o1[i] & ~1u));
      t += dpp<ROR8>(t);
      ov[i] = x16_32_sum(t) * inv;
    }
    if (l < 8) *(LAS f32x4*)(L + L_OV + l * 16) = ov;
  }
  STAMP(6);
  LDSBAR();
  STAMP(8);
  {
    const f32x4 ov = *(LAS f32x4*)(L + L_OV + (l >> 3) * 16);
    float acc[8];
#pragma unroll
    for (int c = 0; c < 8; ++c) acc[c] = 0.f;
#pragma unroll
    for (int i = 0; i < 4; ++i)
#pragma unroll
      for (int c = 0; c < 4; ++c) {
        acc[2 * c] = fmaf(ov[i], bflo(wo[i][c]), acc[2 * c]);
        acc[2 * c + 1] = fmaf(ov[i], bfhi(wo[i][c]), acc[2 * c + 1]);
      }
    float r4[4], r2[2];
#pragma unroll
    for (int c = 0; c < 4; ++c) {
      auto t = __builtin_amdgcn_permlane32_swap(__float_as_uint(acc[c]), __float_as_uint(acc[c + 4]), false, false);
      r4[c] = __uint_as_float(t[0]) + __uint_as_float(t[1]);
    }
#pragma unroll
    for (int c = 0; c < 2; ++c) {
      auto t = __builtin_amdgcn_permlane16_swap(__float_as_uint(r4[c]), __float_as_uint(r4[c + 2]), false, false);
      r2[c] = __uint_as_float(t[0]) + __uint_as_float(t[1]);
    }
    const float u0 = r2[0] + dpp<ROR8>(r2[0]), u1 = r2[1] + dpp<ROR8>(r2[1]);
    const float res = (l & 8) ? u1 : u0;
    const int src = (l >> 3) + ((l & 4) << 3) + ((l & 2) << 3) + ((l & 1) << 3);
    const unsigned colv = (unsigned)__builtin_amdgcn_ds_bpermute(src * 4, (int)san(res));
    for (unsigned spins = 0; __hip_atomic_load((LAS unsigned*)(L + L_TOK), __ATOMIC_RELAXED, __HIP_MEMORY_SCOPE_WORKGROUP) == 0u && spins < (SPIN_CAP << 6); ++spins) { }
    __builtin_amdgcn_raw_buffer_store_b32(colv, rs, WS_IN + (b * 16 + w * 2 + (l >> 5)) * 16384 + (g * 16 + s) * 128 + (l & 31) * 4, 0, AUX_SC1);
  }
  STAMP(9);
  if (cons) {
    const int ai = WS_IN + cidx * 16384 + w * 2048 + l * 16;
    v4u v0, v1;
    for (unsigned spins = 0;; ++spins) {
      asm volatile("" ::: "memory");
      v0 = __builtin_amdgcn_raw_buffer_load_b128(rs, ai, 0, AUX_SC1);
      v1 = __builtin_amdgcn_raw_buffer_load_b128(rs, ai + 1024, 0, AUX_SC1);
      const bool ok = v0[0] != SENT && v0[1] != SENT && v0[2] != SENT && v0[3] != SENT &&
                      v1[0] != SENT && v1[1] != SENT && v1[2] != SENT && v1[3] != SENT;
      if (__all(ok) || spins >= SPIN_CAP) break;
    }
    STAMP(10);
    f32x4 r;
#pragma unroll
    for (int i = 0; i < 4; ++i) {
      float t = __uint_as_float(v0[i]) + __uint_as_float(v1[i]);
      t += dpp<ROR8>(t);
      r[i] = x16_32_sum(t);
    }
    if (l < 8) *(LAS f32x4*)(L + L_FIN + w * 128 + l * 16) = r;
    LDSBAR();
    if (w == 0) {
      const unsigned tag = *(LAS unsigned*)(L + L_TAG);
      float t = 0.f;
#pragma unroll
      for (int ww = 0; ww < 8; ++ww) t += ((LAS float*)(L + L_FIN))[ww * 32 + (l & 31)];
      const float t1 = dpp<XOR1>(t);
      if (l < 32 && !(l & 1)) *(GAS unsigned*)((GAS unsigned char*)out + (cidx * 32 + l) * 2) = cvtpk(t, t1);
      if (l == 0 && g == 0 && s == 0)
        __hip_atomic_store((GAS unsigned*)(ws + WS_EPOCH + b * 256), tag, __ATOMIC_RELAXED, __HIP_MEMORY_SCOPE_AGENT);
    }
    STAMP(11);
  }
  STAMP_FLUSH();
}

extern "C" void kernel_launch(void* const* d_in, const int* in_sizes, int n_in,
                              void* d_out, int out_size, void* d_ws, size_t ws_size,
                              hipStream_t stream) {
  (void)in_sizes; (void)n_in; (void)out_size; (void)ws_size;
  (void)hipFuncSetAttribute((const void*)fused_decode, hipFuncAttributeMaxDynamicSharedMemorySize, LDS_BYTES);
  typedef const unsigned char* P;
  hipLaunchKernelGGL(fused_decode, dim3(NWG), dim3(NTHR), LDS_BYTES, stream,
                     (P)d_in[0], (P)d_in[1], (P)d_in[2], (P)d_in[3], (P)d_in[4],
                     (P)d_in[5], (P)d_in[6], (P)d_in[7], (P)d_in[8], (P)d_in[9],
                     (unsigned char*)d_out, (unsigned char*)d_ws);
}
```

```cpp
#include <hip/hip_runtime.h>
#include <hip/hip_bf16.h>
#include <stdint.h>

#define GAS __attribute__((address_space(1)))
#define LAS __attribute__((address_space(3)))
typedef unsigned v4u __attribute__((ext_vector_type(4)));
typedef unsigned v2u __attribute__((ext_vector_type(2)));
typedef float f32x4 __attribute__((ext_vector_type(4)));
typedef float f32x2 __attribute__((ext_vector_type(2)));
typedef short bf16x8 __attribute__((ext_vector_type(8)));
typedef __bf16 bf16x2 __attribute__((ext_vector_type(2)));
typedef unsigned long long u64;

#define STAMPS 0
#define ADELAY 20
#define ADELAY2 0
#define BDELAY 0
#define BDELAY2 0
#define WOPOS 2
#define X_NOQ 0
#define X_SLOWQ 0
#define X_NOA 0
#define X_NOB 0
#define X_SLOWPV 0
#define X_EXIT 0
#define XROT 1
#define NOACK 0
#define KDELAY 6
#define POS1 2
#define POS2 1

constexpr int HKV = 8, SK = 32768, W = 4096;
constexpr int NWG = 256, NPW = 4, NTHR = (NPW + 8) * 64;
constexpr unsigned SENT = 0xFFFFFFFFu;
constexpr unsigned SPIN_CAP = 1u << 16;
constexpr int AUX_SC1 = 16, AUX_NT = 2;

constexpr int WS_EPOCH = 0;
constexpr int WS_QG = 1024;
constexpr int WS_OR = WS_QG + NWG * 512;
constexpr int WS_IN = WS_OR + NWG * 2048;
constexpr int WS_END = WS_IN + 32 * 16384;
constexpr int WS_STAMP = 2 << 20;
static_assert(WS_OR % 128 == 0 && WS_IN % 128 == 0 && WS_END <= WS_STAMP, "line-aligned records");

constexpr int L_QP = 20480;
constexpr int L_QS = 1024;
constexpr int L_MX = 2048;
constexpr int L_LS = 2176;
constexpr int L_CNT = 2304, L_TOK = 2308, L_SIG = 2312, L_TAG = 2320;
constexpr int L_OV = 2432;
constexpr int L_FIN = 2560;
constexpr int L_OM = 4096;
constexpr int LDS_BYTES = 96 * 1024;

__device__ __forceinline__ float bflo(unsigned u) { return __builtin_bit_cast(float, u << 16); }
__device__ __forceinline__ float bfhi(unsigned u) { return __builtin_bit_cast(float, u & 0xffff0000u); }
__device__ __forceinline__ unsigned cvtpk(float lo, float hi) {
  const f32x2 v = {lo, hi};
  return __builtin_bit_cast(unsigned, __builtin_convertvector(v, bf16x2));
}
template <int CTRL> __device__ __forceinline__ float dpp(float x) {
  return __builtin_bit_cast(float, __builtin_amdgcn_mov_dpp(__builtin_bit_cast(int, x), CTRL, 0xf, 0xf, true));
}
template <int CTRL> __device__ __forceinline__ unsigned dppu(unsigned x) { return (unsigned)__builtin_amdgcn_mov_dpp((int)x, CTRL, 0xf, 0xf, true); }
constexpr int XOR1 = 0xB1, XOR2 = 0x4E, XOR7 = 0x141, ROR8 = 0x128;
__device__ __forceinline__ float fmx(float a, float b) { return __builtin_fmaxf(__builtin_fmaxf(a, b), -__builtin_inff()); }
__device__ __forceinline__ float fmx3(float a, float b, float c) { return __builtin_fmaxf(__builtin_fmaxf(a, b), c); }
__device__ __forceinline__ float tr32(float a, float b) { auto t = __builtin_amdgcn_permlane32_swap(__float_as_uint(a), __float_as_uint(b), false, false); return __uint_as_float(t[0]) + __uint_as_float(t[1]); }
__device__ __forceinline__ float tr16(float a, float b) { auto t = __builtin_amdgcn_permlane16_swap(__float_as_uint(a), __float_as_uint(b), false, false); return __uint_as_float(t[0]) + __uint_as_float(t[1]); }
__device__ __forceinline__ float x16_32_max(float x) {
  auto s = __builtin_amdgcn_permlane16_swap(__float_as_uint(x), __float_as_uint(x), false, false);
  x = fmx(__uint_as_float(s[0]), __uint_as_float(s[1]));
  auto t = __builtin_amdgcn_permlane32_swap(__float_as_uint(x), __float_as_uint(x), false, false);
  return fmx(__uint_as_float(t[0]), __uint_as_float(t[1]));
}
__device__ __forceinline__ float tr_reduce8(const float (&a)[8], int l) {
  const float r0 = tr16(tr32(a[0], a[4]), tr32(a[2], a[6])), r1 = tr16(tr32(a[1], a[5]), tr32(a[3], a[7]));
  const float u0 = r0 + dpp<ROR8>(r0), u1 = r1 + dpp<ROR8>(r1);
  return (l & 8) ? u1 : u0;
}
__device__ __forceinline__ float tr_reduce4(float a0, float a1, float a2, float a3) {
  const float r = tr16(tr32(a0, a2), tr32(a1, a3));
  return r + dpp<ROR8>(r);
}
__device__ __forceinline__ f32x4 mfma(v4u a, v4u b, f32x4 c) {
  return __builtin_amdgcn_mfma_f32_16x16x32_bf16(__builtin_bit_cast(bf16x8, a), __builtin_bit_cast(bf16x8, b), c, 0, 0, 0);
}
__device__ __forceinline__ unsigned umax3(unsigned a, unsigned b, unsigned c) { return max(max(a, b), c); }
__device__ __forceinline__ unsigned san(unsigned u) { return min(u, 0xFFFFFFFEu); }
__device__ __forceinline__ bool none_sent(const v4u& a, const v4u& b) {
  const unsigned m = max(umax3(umax3(a[0], a[1], a[2]), a[3], b[0]), umax3(b[1], b[2], b[3]));
  return __builtin_amdgcn_ballot_w64(m == SENT) == 0ull;
}
template <int D1, int D2>
__device__ __forceinline__ void poll2(__amdgpu_buffer_rsrc_t rs, int a0, int a1, v4u& o0, v4u& o1) {
#define P2_ISSUE(x, y) do { asm volatile("" ::: "memory"); x = __builtin_amdgcn_raw_buffer_load_b128(rs, a0, 0, AUX_SC1); y = __builtin_amdgcn_raw_buffer_load_b128(rs, a1, 0, AUX_SC1); \
    __builtin_amdgcn_sched_barrier(0); } while (0)
  if (D1) { __builtin_amdgcn_s_sleep(D1); __builtin_amdgcn_sched_barrier(0); }
  if (D2) {
    v4u x0, y0, x1, y1;
    P2_ISSUE(x0, y0);
    __builtin_amdgcn_s_sleep(D2); __builtin_amdgcn_sched_barrier(0);
    P2_ISSUE(x1, y1);
    if (none_sent(x0, y0)) { o0 = x0; o1 = y0; return; }
    if (none_sent(x1, y1)) { o0 = x1; o1 = y1; return; }
  }
  for (unsigned spins = 0;; ++spins) {
    P2_ISSUE(o0, o1);
    if (none_sent(o0, o1) || spins >= SPIN_CAP) return;
  }
#undef P2_ISSUE
}
#define LDSBAR() do { asm volatile("s_waitcnt lgkmcnt(0)" ::: "memory"); __builtin_amdgcn_s_barrier(); asm volatile("" ::: "memory"); } while (0)
#define SB() __builtin_amdgcn_sched_barrier(0)
#define STAMP(i) do { } while (0)
#define STAMP_FLUSH() do { } while (0)
__device__ __forceinline__ __amdgpu_buffer_rsrc_t mkrs(const void* p, int bytes) { return __builtin_amdgcn_make_buffer_rsrc((void*)p, 0, bytes, 0x00020000); }
template <int AUX> __device__ __forceinline__ v4u bld(__amdgpu_buffer_rsrc_t r, int voff, int soff) { return __builtin_amdgcn_raw_buffer_load_b128(r, voff, soff, AUX); }

__global__ void __launch_bounds__(NTHR, 3)
fused_decode(const unsigned char* __restrict__ xA, const unsigned char* __restrict__ kA, const unsigned char* __restrict__ vA,
             const unsigned char* __restrict__ wqA, const unsigned char* __restrict__ woA,
             const unsigned char* __restrict__ xB, const unsigned char* __restrict__ kB, const unsigned char* __restrict__ vB,
             const unsigned char* __restrict__ wqB, const unsigned char* __restrict__ woB,
             unsigned char* __restrict__ out, unsigned char* ws_) {
  asm volatile("" :: "s"(xA), "s"(kA), "s"(vA), "s"(wqA), "s"(woA), "s"(xB), "s"(kB), "s"(vB), "s"(wqB), "s"(woB), "s"(out), "s"(ws_));
  extern __shared__ __attribute__((aligned(16))) unsigned char smem[];
  LAS unsigned char* L = (LAS unsigned char*)smem;
  GAS unsigned char* ws = (GAS unsigned char*)ws_;
  const int tid = threadIdx.x, l = tid & 63;
  const int wv = __builtin_amdgcn_readfirstlane(tid >> 6);
  const int bid = blockIdx.x, g = bid & 7, j = bid >> 3, b = j >> 4, s = j & 15;
  const int grp = b * 8 + g, me = grp * 16 + s;
  const int cidx = b * 16 + g * 2 + (s >> 3);
  const __amdgpu_buffer_rsrc_t rs = mkrs(ws_, WS_END);
  STAMP(0);

  if (X_EXIT == 1) return;
  if (X_EXIT >= 100) { const u64 t0_ = __builtin_amdgcn_s_memrealtime(); while (__builtin_amdgcn_s_memrealtime() - t0_ < (u64)(X_EXIT)) __builtin_amdgcn_s_sleep(1); return; }
  if (wv < NPW) {
    __builtin_amdgcn_s_setprio(3);
    const int p = wv;
    if (tid == 0) *(LAS v4u*)(L + L_CNT) = v4u{0u, 0u, 0u, 0u};
    const __amdgpu_buffer_rsrc_t rx = mkrs(b ? xB : xA, 1024);
    const __amdgpu_buffer_rsrc_t rq = mkrs(b ? wqB : wqA, 512 * 8192);
    const v4u xv = bld<0>(rx, (l >> 4) * 16, (s & 3) * 256 + p * 64);
    v4u wq[8];
    {
      const int vo = (l >> 4) * 8 * 8192 + (l & 15) * 16, so = ((s & 3) * 128 + p * 32) * 8192 + (g * 512 + (s >> 2) * 128) * 2;
#pragma unroll
      for (int i = 0; i < 8; ++i) wq[i] = bld<AUX_NT>(rq, vo, so + i * 8192);
    }
    SB();
    const unsigned ep = __hip_atomic_load((GAS unsigned*)(ws + WS_EPOCH + b * 256), __ATOMIC_RELAXED, __HIP_MEMORY_SCOPE_AGENT);
    __builtin_amdgcn_raw_buffer_store_b128(v4u{SENT, SENT, SENT, SENT}, rs,
                                           tid < 128 ? WS_OR + me * 2048 + tid * 16 : WS_IN + cidx * 16384 + (s & 7) * 2048 + (tid - 128) * 16, 0, AUX_SC1);
    SB();
    if (KDELAY == 0) { LDSBAR(); SB(); }
    f32x2 a2[4];
#pragma unroll
    for (int i = 0; i < 8; ++i) {
      const float xs = (i & 1) ? bfhi(xv[i >> 1]) : bflo(xv[i >> 1]);
      const f32x2 x2 = {xs, xs};
#pragma unroll
      for (int c = 0; c < 4; ++c) {
        const f32x2 w2 = {bflo(wq[i][c]), bfhi(wq[i][c])};
        a2[c] = i == 0 ? x2 * w2 : __builtin_elementwise_fma(x2, w2, a2[c]);
      }
      if (KDELAY == i + 1) { asm volatile("" :: "v"(a2[0]), "v"(a2[3])); SB(); LDSBAR(); SB(); }
    }
    STAMP(1);
    {
      const f32x2 r = {tr16(tr32(a2[0][0], a2[2][0]), tr32(a2[1][0], a2[3][0])), tr16(tr32(a2[0][1], a2[2][1]), tr32(a2[1][1], a2[3][1]))};
      *(LAS f32x2*)(L + L_QP + p * 512 + (l & 15) * 32 + (l >> 5) * 16 + ((l >> 4) & 1) * 8) = r;
    }
    asm volatile("s_waitcnt vmcnt(0) lgkmcnt(0)" ::: "memory");
    unsigned arr = 0;
    if (l == 0) arr = __hip_atomic_fetch_add((LAS unsigned*)(L + L_CNT), 1u, __ATOMIC_RELAXED, __HIP_MEMORY_SCOPE_WORKGROUP);
    arr = __builtin_amdgcn_readfirstlane(arr);
    const unsigned tag = ep + 1u;
    if (arr == NPW - 1) {
      asm volatile("" ::: "memory");
      f32x2 q = *(volatile LAS f32x2*)(L + L_QP + l * 8);
#pragma unroll
      for (int ww = 1; ww < NPW; ++ww) q += *(volatile LAS f32x2*)(L + L_QP + ww * 512 + l * 8);
      __hip_atomic_store((GAS u64*)(ws + WS_QG) + me * 64 + l, ((u64)tag << 32) | cvtpk(q[0], q[1]), __ATOMIC_RELAXED, __HIP_MEMORY_SCOPE_AGENT);
    }
    STAMP(3);
    if (p == 0) {
      const int qo = WS_QG + grp * 8192 + l * 16;
      v4u qa[8], qb[8], r[8];
      auto sig = [&](unsigned m) {
        for (unsigned spins = 0; !(__hip_atomic_load((LAS unsigned*)(L + L_SIG), __ATOMIC_RELAXED, __HIP_MEMORY_SCOPE_WORKGROUP) & m) && spins < (SPIN_CAP << 4); ++spins) __builtin_amdgcn_s_sleep(1);
      };
      auto tagged = [&](const v4u (&a)[8]) -> bool {
        unsigned d = 0;
#pragma unroll
        for (int i = 0; i < 8; ++i) d |= (a[i][1] ^ tag) | (a[i][3] ^ tag);
        return __builtin_amdgcn_ballot_w64(d != 0u) == 0ull;
      };
      if (!X_NOQ) sig(1u);
      asm volatile("" ::: "memory");
#pragma unroll
      for (int i = 0; i < 8; ++i) qa[i] = __builtin_amdgcn_raw_buffer_load_b128(rs, qo + i * 1024, 0, AUX_SC1);
      SB();
      if (!X_NOQ) sig(2u);
      asm volatile("" ::: "memory");
#pragma unroll
      for (int i = 0; i < 8; ++i) qb[i] = __builtin_amdgcn_raw_buffer_load_b128(rs, qo + i * 1024, 0, AUX_SC1);
      SB();
      bool done = X_NOQ ? true : tagged(qa);
#pragma unroll
      for (int i = 0; i < 8; ++i) r[i] = qa[i];
      STAMP(9);
      if (!done) {
        done = tagged(qb);
#pragma unroll
        for (int i = 0; i < 8; ++i) r[i] = qb[i];
        STAMP(10);
        for (unsigned spins = 0; !done && spins < SPIN_CAP; ++spins) {
          asm volatile("" ::: "memory");
#pragma unroll
          for (int i = 0; i < 8; ++i) r[i] = __builtin_amdgcn_raw_buffer_load_b128(rs, qo + i * 1024, 0, AUX_SC1);
          done = tagged(r);
        }
      }
#pragma unroll
      for (int h = 0; h < 4; ++h) {
        const v4u u = r[2 * h], v = r[2 * h + 1];
        const float a = tr32(bflo(u[0]) + bflo(v[0]), bflo(u[2]) + bflo(v[2])), c = tr32(bfhi(u[0]) + bfhi(v[0]), bfhi(u[2]) + bfhi(v[2]));
        *(LAS unsigned*)(L + L_QS + h * 256 + (l & 31) * 8 + (l >> 5) * 4) = cvtpk(a, c);
      }
      if (X_SLOWQ) __builtin_amdgcn_s_sleep(X_SLOWQ);
      if (l == 0) *(LAS unsigned*)(L + L_TAG) = tag;
      STAMP(4);
    }
    LDSBAR();
    if (p == 0) {
      GAS u64* tk = (GAS u64*)(ws + WS_QG) + ((b * 8 + ((l & 15) >> 1)) * 16 + (l & 1) * 8 + g) * 64;
      for (unsigned spins = 0;; ++spins) {
        const u64 tx = __hip_atomic_load(tk, __ATOMIC_RELAXED, __HIP_MEMORY_SCOPE_AGENT);
        if (__all((unsigned)(tx >> 32) == tag) || spins >= SPIN_CAP) break;
      }
      if (l == 0) __hip_atomic_store((LAS unsigned*)(L + L_TOK), 1u, __ATOMIC_RELAXED, __HIP_MEMORY_SCOPE_WORKGROUP);
    }
    STAMP_FLUSH();
    return;
  }

  const int w = wv - NPW;
  const __amdgpu_buffer_rsrc_t rk = mkrs(b ? kB : kA, HKV * SK * 256);
  const __amdgpu_buffer_rsrc_t rv = mkrs(b ? vB : vA, HKV * SK * 256);
  const __amdgpu_buffer_rsrc_t ro = mkrs(b ? woB : woA, 4096 * 1024);
  const int kvbase = (g * SK + (SK - W) + s * 256 + w * 32) * 256;
  const int rot = XROT ? (w + s) & 7 : 0;
  const int kvo = ((l & 15) >> 2) * 256 + (l >> 4) * 64 + (l & 3) * 16;
  __builtin_amdgcn_s_setprio(2);
  LDSBAR();
  SB();
  v4u kf[8], vf[8];
#pragma unroll
  for (int m = 0; m < 8; ++m) kf[m] = bld<AUX_NT>(rk, kvo, kvbase + (m ^ rot) * 1024);
  SB();
  __builtin_amdgcn_s_setprio(1);
#pragma unroll
  for (int m = 0; m < 8; ++m) {
    vf[m] = bld<AUX_NT>(rv, l * 16, kvbase + (m ^ rot) * 1024);
    SB();
    if (m + 1 == POS1 || m + 1 == POS2) {
      if (l == 0 && ((m + 1 == POS1 && w == 0) || (m + 1 == POS2 && w == 4)))
        __hip_atomic_fetch_or((LAS unsigned*)(L + L_SIG), w == 0 ? 1u : 2u, __ATOMIC_RELAXED, __HIP_MEMORY_SCOPE_WORKGROUP);
      SB();
    }
  }
  v4u wo[4];
#define WO_LOADS() do { __builtin_amdgcn_s_setprio(0); \
    const int vo = (l >> 3) * 4 * 1024 + (l & 7) * 16, so = (g * 512 + s * 32) * 1024 + w * 128; \
    _Pragma("unroll") for (int i = 0; i < 4; ++i) wo[i] = bld<AUX_NT>(ro, vo + i * 1024, so); \
    SB(); } while (0)
  if (WOPOS == 1) WO_LOADS();
  STAMP(1);
  LDSBAR();
  STAMP(2);
  const v4u qf = *(LAS v4u*)(L + L_QS + kvo);
  float sc[8];
#pragma unroll
  for (int m = 0; m < 8; ++m) {
    const f32x4 d = mfma(kf[m], qf, f32x4{0.f, 0.f, 0.f, 0.f});
    sc[m] = (dpp<0x00>(d[0]) + dpp<0x55>(d[1])) + (dpp<0xAA>(d[2]) + dpp<0xFF>(d[3]));
  }
  constexpr float C2 = 0.088388347648318440f * 1.4426950408889634f;
  const float Mc = x16_32_max(fmx(fmx3(fmx3(sc[0], sc[1], sc[2]), sc[3], sc[4]), fmx3(sc[5], sc[6], sc[7]))) * C2;
  if (l < 16 && (l & 3) == 0) ((LAS float*)(L + L_MX))[(l >> 2) * 8 + w] = Mc;
  v4u pf;
  {
    float p[8];
#pragma unroll
    for (int m = 0; m < 8; ++m) p[m] = __builtin_amdgcn_exp2f(fmaf(sc[m], C2, -Mc));
    pf = v4u{cvtpk(p[0], p[1]), cvtpk(p[2], p[3]), cvtpk(p[4], p[5]), cvtpk(p[6], p[7])};
  }
  const v4u ones = {0x3F803F80u, 0x3F803F80u, 0x3F803F80u, 0x3F803F80u};
  const f32x4 lsum = mfma(pf, ones, f32x4{0.f, 0.f, 0.f, 0.f});
  if ((l & 15) == 0) ((LAS float*)(L + L_LS))[(l >> 4) * 8 + w] = lsum[0];
  STAMP(3);
  LDSBAR();
  float ew[8]; unsigned nib = 0;
  if (w < 2) {
    const int h = (w * 64 + l) >> 5;
    const f32x4 m0 = *(LAS f32x4*)(L + L_MX + h * 32), m1 = *(LAS f32x4*)(L + L_MX + h * 32 + 16);
    const f32x4 l0 = *(LAS f32x4*)(L + L_LS + h * 32), l1 = *(LAS f32x4*)(L + L_LS + h * 32 + 16);
    const float Mh = fmx(fmx3(fmx3(m0[0], m0[1], m0[2]), m0[3], m1[0]), fmx3(m1[1], m1[2], m1[3]));
    float Lh = 0.f;
#pragma unroll
    for (int ww = 0; ww < 8; ++ww) { ew[ww] = __builtin_amdgcn_exp2f((ww < 4 ? m0[ww & 3] : m1[ww & 3]) - Mh); Lh = fmaf(ew[ww], ww < 4 ? l0[ww & 3] : l1[ww & 3], Lh); }
    const float inv = __builtin_amdgcn_rcpf(Lh);
#pragma unroll
    for (int ww = 0; ww < 8; ++ww) { ew[ww] *= inv; asm volatile("" : "+v"(ew[ww])); }
    nib = (__float_as_uint(Mh + __builtin_amdgcn_logf(Lh)) >> ((l & 7) * 4)) & 15u;
    asm volatile("" : "+v"(nib));
  }
  SB();
  if (WOPOS == 2) WO_LOADS();
  {
    v4u bfr[8];
#pragma unroll
    for (int u = 0; u < 4; ++u) {
      if (u == 3) {
        STAMP(4); SB(); asm volatile("" :: "v"(vf[7])); SB(); STAMP(5);
        if (WOPOS == 0) WO_LOADS();
      }
#pragma unroll
      for (int d = 0; d < 8; ++d) bfr[d][u] = __builtin_amdgcn_perm(vf[2 * u + 1][d >> 1], vf[2 * u][d >> 1], (d & 1) ? 0x07060302u : 0x05040100u);
      if (u < 3) SB();
    }
    float o[8];
#pragma unroll
    for (int d = 0; d < 8; ++d) o[d] = mfma(pf, bfr[d], f32x4{0.f, 0.f, 0.f, 0.f})[0];
    LAS f32x4* om = (LAS f32x4*)(L + L_OM + w * 2048 + l * 16);
    om[0] = f32x4{o[0], o[1], o[2], o[3]};
    om[64] = f32x4{o[4], o[5], o[6], o[7]};
  }
  if (X_EXIT == 2) { asm volatile("" :: "v"(wo[0]), "v"(wo[1]), "v"(wo[2]), "v"(wo[3])); return; }
  if (X_EXIT == 6) { return; }
  if (X_SLOWPV) __builtin_amdgcn_s_sleep(X_SLOWPV);
  STAMP(6);
  LDSBAR();
  STAMP(7);
  if (w < 2) {
    const int t = w * 64 + l;
    const int lo = (t >> 5) * 16 + ((t & 31) >> 1), h2 = t & 1;
    f32x4 a = *(LAS f32x4*)(L + L_OM + h2 * 1024 + lo * 16) * ew[0];
#pragma unroll
    for (int ww = 1; ww < 8; ++ww) a += *(LAS f32x4*)(L + L_OM + ww * 2048 + h2 * 1024 + lo * 16) * ew[ww];
    const v4u o4 = {san((__float_as_uint(a[0]) & ~15u) | nib), san(__float_as_uint(a[1])), san(__float_as_uint(a[2])), san(__float_as_uint(a[3]))};
    __builtin_amdgcn_raw_buffer_store_b128(o4, rs, WS_OR + me * 2048 + t * 16, 0, AUX_SC1);
    STAMP(8);
  } else if (w == 3) {
    const int a0 = WS_OR + (grp * 16 + (l >> 3)) * 2048 + s * 128 + (l & 7) * 16;
    v4u o0, o1;
    if (X_NOA) { o0 = __builtin_amdgcn_raw_buffer_load_b128(rs, a0, 0, AUX_SC1); o1 = __builtin_amdgcn_raw_buffer_load_b128(rs, a0 + 8 * 2048, 0, AUX_SC1); } else
    poll2<ADELAY, ADELAY2>(rs, a0, a0 + 8 * 2048, o0, o1);
    STAMP(9);
    unsigned ea = (o0[0] & 15u) << ((l & 7) * 4), eb = (o1[0] & 15u) << ((l & 7) * 4);
    ea |= dppu<XOR1>(ea); eb |= dppu<XOR1>(eb);
    ea |= dppu<XOR2>(ea); eb |= dppu<XOR2>(eb);
    ea |= dppu<XOR7>(ea); eb |= dppu<XOR7>(eb);
    const float ma = __uint_as_float(ea), mb = __uint_as_float(eb);
    float Mg = fmx(ma, mb);
    Mg = x16_32_max(fmx(Mg, dpp<ROR8>(Mg)));
    const float wa = __builtin_amdgcn_exp2f(ma - Mg), wb = __builtin_amdgcn_exp2f(mb - Mg);
    const float o_0 = fmaf(wa, __uint_as_float(o0[0] & ~15u), wb * __uint_as_float(o1[0] & ~15u));
    const float o_1 = fmaf(wa, __uint_as_float(o0[1]), wb * __uint_as_float(o1[1]));
    const float o_2 = fmaf(wa, __uint_as_float(o0[2]), wb * __uint_as_float(o1[2]));
    const float o_3 = fmaf(wa, __uint_as_float(o0[3]), wb * __uint_as_float(o1[3]));
    float Lg = wa + wb;
    Lg += dpp<ROR8>(Lg);
    Lg = tr16(Lg, Lg); Lg = tr32(Lg, Lg);
    const float ov = tr_reduce4(o_0, o_1, o_2, o_3) * __builtin_amdgcn_rcpf(Lg);
    if (!(l & 8)) ((LAS float*)(L + L_OV))[(l & 7) * 4 + (l >> 5) * 2 + ((l >> 4) & 1)] = ov;
  }
  if (X_EXIT == 4) return;
  if (w != 3) for (unsigned spins = 0; __hip_atomic_load((LAS unsigned*)(L + L_TOK), __ATOMIC_RELAXED, __HIP_MEMORY_SCOPE_WORKGROUP) == 0u && spins < (SPIN_CAP << 6); ++spins) { }
  LDSBAR();
  STAMP(10);
  {
    const f32x4 ov = *(LAS f32x4*)(L + L_OV + (l >> 3) * 16);
    f32x2 a2[4];
#pragma unroll
    for (int i = 0; i < 4; ++i) {
      const f32x2 o2 = {ov[i], ov[i]};
#pragma unroll
      for (int c = 0; c < 4; ++c) {
        const f32x2 w2 = {bflo(wo[i][c]), bfhi(wo[i][c])};
        a2[c] = i == 0 ? o2 * w2 : __builtin_elementwise_fma(o2, w2, a2[c]);
      }
    }
    const float acc[8] = {a2[0][0], a2[0][1], a2[1][0], a2[1][1], a2[2][0], a2[2][1], a2[3][0], a2[3][1]};
    const float res = tr_reduce8(acc, l);
    const int from = (l >> 3) + ((l & 7) << 3);
    const unsigned colv = (unsigned)__builtin_amdgcn_ds_bpermute(from * 4, (int)san(__float_as_uint(res)));
    __builtin_amdgcn_raw_buffer_store_b32(colv, rs, WS_IN + (b * 16 + w * 2 + (l >> 5)) * 16384 + (g * 16 + s) * 128 + (l & 31) * 4, 0, AUX_SC1);
  }
  if (X_EXIT == 5) return;
  STAMP(11);
  if ((s & 7) == 0) {
    const int ai = WS_IN + cidx * 16384 + w * 2048 + l * 16;
    v4u v0, v1;
    if (X_NOB) { v0 = __builtin_amdgcn_raw_buffer_load_b128(rs, ai, 0, AUX_SC1); v1 = __builtin_amdgcn_raw_buffer_load_b128(rs, ai + 1024, 0, AUX_SC1); } else
    poll2<BDELAY, BDELAY2>(rs, ai, ai + 1024, v0, v1);
    STAMP(12);
    const float r = tr_reduce4(__uint_as_float(v0[0]) + __uint_as_float(v1[0]), __uint_as_float(v0[1]) + __uint_as_float(v1[1]),
                               __uint_as_float(v0[2]) + __uint_as_float(v1[2]), __uint_as_float(v0[3]) + __uint_as_float(v1[3]));
    if (!(l & 8)) ((LAS float*)(L + L_FIN))[w * 32 + (l & 7) * 4 + (l >> 5) * 2 + ((l >> 4) & 1)] = r;
    LDSBAR();
    STAMP(13);
    if (w == 1 && l == 0 && g == 0 && s == 0)
      __hip_atomic_store((GAS unsigned*)(ws + WS_EPOCH + b * 256), *(LAS unsigned*)(L + L_TAG), __ATOMIC_RELAXED, __HIP_MEMORY_SCOPE_AGENT);
    if (w == 0) {
      float t = 0.f;
#pragma unroll
      for (int ww = 0; ww < 8; ++ww) t += ((LAS float*)(L + L_FIN))[ww * 32 + (l & 31)];
      const float t1 = dpp<XOR1>(t);
      if (l < 32 && !(l & 1)) *(GAS unsigned*)((GAS unsigned char*)out + (cidx * 32 + l) * 2) = cvtpk(t, t1);
    }
    STAMP(14);
  }
  STAMP_FLUSH();
}

extern "C" void kernel_launch(void* const* d_in, const int* in_sizes, int n_in,
                              void* d_out, int out_size, void* d_ws, size_t ws_size,
                              hipStream_t stream) {
  (void)in_sizes; (void)n_in; (void)out_size; (void)ws_size;
  (void)hipFuncSetAttribute((const void*)fused_decode, hipFuncAttributeMaxDynamicSharedMemorySize, LDS_BYTES);
  typedef const unsigned char* P;
  hipLaunchKernelGGL(fused_decode, dim3(NWG), dim3(NTHR), LDS_BYTES, stream,
                     (P)d_in[0], (P)d_in[1], (P)d_in[2], (P)d_in[3], (P)d_in[4],
                     (P)d_in[5], (P)d_in[6], (P)d_in[7], (P)d_in[8], (P)d_in[9],
                     (unsigned char*)d_out, (unsigned char*)d_ws);
}
```

```cpp
#include <hip/hip_runtime.h>
#include <hip/hip_bf16.h>
#include <stdint.h>

#define GAS __attribute__((address_space(1)))
#define LAS __attribute__((address_space(3)))
typedef unsigned v4u __attribute__((ext_vector_type(4)));
typedef unsigned v2u __attribute__((ext_vector_type(2)));
typedef float f32x4 __attribute__((ext_vector_type(4)));
typedef float f32x2 __attribute__((ext_vector_type(2)));
typedef short bf16x8 __attribute__((ext_vector_type(8)));
typedef __bf16 bf16x2 __attribute__((ext_vector_type(2)));
typedef unsigned long long u64;

#define STAMPS 0
#define X_EXIT 0
#define KDELAY 2
#define POS1 4
#define POS2 2
#define ADELAY 22
#define BDELAY 0

constexpr int HKV = 8, SK = 32768, W = 4096;
constexpr int NWG = 256, NPW = 4, NTHR = (NPW + 8) * 64;
constexpr unsigned SPIN_CAP = 1u << 16;
constexpr int AUX_SC1 = 16, AUX_NT = 2, OOB = 0x7FFF0000;

constexpr int WS_EPOCH = 0;
constexpr int WS_QG = 1024;
constexpr int WS_OR = WS_QG + NWG * 512;
constexpr int WS_IN = WS_OR + NWG * 2048;
constexpr int WS_END = WS_IN + 32 * 16384;
constexpr int WS_STAMP = 2 << 20;
static_assert(WS_OR % 128 == 0 && WS_IN % 128 == 0 && WS_END <= WS_STAMP, "line-aligned records");

constexpr int L_QS = 1024;
constexpr int L_MX = 2048;
constexpr int L_LS = 2176;
constexpr int L_CNT = 2304, L_SIG = 2312, L_MLC = 2316;
constexpr int L_TAG = 2368;
constexpr int L_OV = 2432;
constexpr int L_FIN = 2560;
constexpr int L_OM = 4096;
constexpr int L_QP = 20480;
constexpr int LDS_BYTES = 96 * 1024;

__device__ __forceinline__ float bflo(unsigned u) { return __builtin_bit_cast(float, u << 16); }
__device__ __forceinline__ float bfhi(unsigned u) { return __builtin_bit_cast(float, u & 0xffff0000u); }
__device__ __forceinline__ unsigned cvtpk(float lo, float hi) {
  const f32x2 v = {lo, hi};
  return __builtin_bit_cast(unsigned, __builtin_convertvector(v, bf16x2));
}
template <int CTRL> __device__ __forceinline__ float dpp(float x) {
  return __builtin_bit_cast(float, __builtin_amdgcn_mov_dpp(__builtin_bit_cast(int, x), CTRL, 0xf, 0xf, true));
}
template <int CTRL> __device__ __forceinline__ unsigned dppu(unsigned x) { return (unsigned)__builtin_amdgcn_mov_dpp((int)x, CTRL, 0xf, 0xf, true); }
constexpr int XOR1 = 0xB1, XOR2 = 0x4E, XOR7 = 0x141, ROR8 = 0x128;
__device__ __forceinline__ float fmx(float a, float b) { return __builtin_fmaxf(__builtin_fmaxf(a, b), -__builtin_inff()); }
__device__ __forceinline__ float fmx3(float a, float b, float c) { return __builtin_fmaxf(__builtin_fmaxf(a, b), c); }
__device__ __forceinline__ float tr32(float a, float b) { auto t = __builtin_amdgcn_permlane32_swap(__float_as_uint(a), __float_as_uint(b), false, false); return __uint_as_float(t[0]) + __uint_as_float(t[1]); }
__device__ __forceinline__ float tr16(float a, float b) { auto t = __builtin_amdgcn_permlane16_swap(__float_as_uint(a), __float_as_uint(b), false, false); return __uint_as_float(t[0]) + __uint_as_float(t[1]); }
__device__ __forceinline__ float x16_32_max(float x) {
  auto s = __builtin_amdgcn_permlane16_swap(__float_as_uint(x), __float_as_uint(x), false, false);
  x = fmx(__uint_as_float(s[0]), __uint_as_float(s[1]));
  auto t = __builtin_amdgcn_permlane32_swap(__float_as_uint(x), __float_as_uint(x), false, false);
  return fmx(__uint_as_float(t[0]), __uint_as_float(t[1]));
}
__device__ __forceinline__ float tr_reduce8(const float (&a)[8], int l) {
  const float r0 = tr16(tr32(a[0], a[4]), tr32(a[2], a[6])), r1 = tr16(tr32(a[1], a[5]), tr32(a[3], a[7]));
  const float u0 = r0 + dpp<ROR8>(r0), u1 = r1 + dpp<ROR8>(r1);
  return (l & 8) ? u1 : u0;
}
__device__ __forceinline__ float tr_reduce4(float a0, float a1, float a2, float a3) {
  const float r = tr16(tr32(a0, a2), tr32(a1, a3));
  return r + dpp<ROR8>(r);
}
__device__ __forceinline__ f32x4 mfma(v4u a, v4u b, f32x4 c) {
  return __builtin_amdgcn_mfma_f32_16x16x32_bf16(__builtin_bit_cast(bf16x8, a), __builtin_bit_cast(bf16x8, b), c, 0, 0, 0);
}
template <unsigned M0, unsigned M> __device__ __forceinline__ unsigned tagdiff(const v4u& a, const v4u& f) {
  return (((a[0] ^ f[0]) & M0) | ((a[1] ^ f[1]) & M)) | (((a[2] ^ f[2]) & M) | ((a[3] ^ f[3]) & M));
}
#define LDSBAR() do { asm volatile("s_waitcnt lgkmcnt(0)" ::: "memory"); __builtin_amdgcn_s_barrier(); asm volatile("" ::: "memory"); } while (0)
#define SB() __builtin_amdgcn_sched_barrier(0)
template <int DELAY, unsigned M0, unsigned M>
__device__ __forceinline__ void poll2(__amdgpu_buffer_rsrc_t rs, int a0, int a1, const v4u& f, v4u& o0, v4u& o1) {
  if (DELAY) { __builtin_amdgcn_s_sleep(DELAY); SB(); }
  for (unsigned spins = 0;; ++spins) {
    asm volatile("" ::: "memory");
    o0 = __builtin_amdgcn_raw_buffer_load_b128(rs, a0, 0, AUX_SC1);
    o1 = __builtin_amdgcn_raw_buffer_load_b128(rs, a1, 0, AUX_SC1);
    if (__builtin_amdgcn_ballot_w64((tagdiff<M0, M>(o0, f) | tagdiff<M0, M>(o1, f)) != 0u) == 0ull || spins >= SPIN_CAP) return;
  }
}
#define ASTAMP(i) do { } while (0)
#define STAMP(i) do { } while (0)
#define STAMP_FLUSH() do { } while (0)
__device__ __forceinline__ __amdgpu_buffer_rsrc_t mkrs(const void* p, int bytes) { return __builtin_amdgcn_make_buffer_rsrc((void*)p, 0, bytes, 0x00020000); }
template <int AUX> __device__ __forceinline__ v4u bld(__amdgpu_buffer_rsrc_t r, int voff, int soff) { return __builtin_amdgcn_raw_buffer_load_b128(r, voff, soff, AUX); }

__global__ void __launch_bounds__(NTHR, 3)
fused_decode(const unsigned char* __restrict__ xA, const unsigned char* __restrict__ kA, const unsigned char* __restrict__ vA,
             const unsigned char* __restrict__ wqA, const unsigned char* __restrict__ woA,
             const unsigned char* __restrict__ xB, const unsigned char* __restrict__ kB, const unsigned char* __restrict__ vB,
             const unsigned char* __restrict__ wqB, const unsigned char* __restrict__ woB,
             unsigned char* __restrict__ out, unsigned char* ws_) {
  asm volatile("" :: "s"(xA), "s"(kA), "s"(vA), "s"(wqA), "s"(woA), "s"(xB), "s"(kB), "s"(vB), "s"(wqB), "s"(woB), "s"(out), "s"(ws_));
  extern __shared__ __attribute__((aligned(16))) unsigned char smem[];
  LAS unsigned char* L = (LAS unsigned char*)smem;
  GAS unsigned char* ws = (GAS unsigned char*)ws_;
  const int tid = threadIdx.x, l = tid & 63;
  const int wv = __builtin_amdgcn_readfirstlane(tid >> 6);
  const int bid = blockIdx.x, g = bid & 7, j = bid >> 3, b = j >> 4, s = j & 15;
  const int grp = b * 8 + g, me = grp * 16 + s;
  const __amdgpu_buffer_rsrc_t rs = mkrs(ws_, WS_END);
  STAMP(0);
  if (X_EXIT >= 100) { const u64 t0_ = __builtin_amdgcn_s_memrealtime(); while (__builtin_amdgcn_s_memrealtime() - t0_ < (u64)(X_EXIT)) __builtin_amdgcn_s_sleep(1); return; }

  if (wv < NPW) {
    __builtin_amdgcn_s_setprio(3);
    const int p = wv;
    if (tid == 0) *(LAS v4u*)(L + L_CNT) = v4u{0u, 0u, 0u, 0u};
    const __amdgpu_buffer_rsrc_t rx = mkrs(b ? xB : xA, 1024);
    const __amdgpu_buffer_rsrc_t rq = mkrs(b ? wqB : wqA, 512 * 8192);
    const v4u xv = bld<0>(rx, (l >> 4) * 16, (s & 3) * 256 + p * 64);
    v4u wq[8];
    {
      const int vo = (l >> 4) * 8 * 8192 + (l & 15) * 16, so = ((s & 3) * 128 + p * 32) * 8192 + (g * 512 + (s >> 2) * 128) * 2;
#pragma unroll
      for (int i = 0; i < 8; ++i) wq[i] = bld<AUX_NT>(rq, vo, so + i * 8192);
    }
    SB();
    const u64 epv = __hip_atomic_load((GAS u64*)(ws + WS_EPOCH + b * 256), __ATOMIC_RELAXED, __HIP_MEMORY_SCOPE_AGENT);
    SB();
    if (KDELAY == 0) { LDSBAR(); SB(); }
    f32x2 a2[4];
#pragma unroll
    for (int i = 0; i < 8; ++i) {
      const float xs = (i & 1) ? bfhi(xv[i >> 1]) : bflo(xv[i >> 1]);
      const f32x2 x2 = {xs, xs};
#pragma unroll
      for (int c = 0; c < 4; ++c) {
        const f32x2 w2 = {bflo(wq[i][c]), bfhi(wq[i][c])};
        a2[c] = i == 0 ? x2 * w2 : __builtin_elementwise_fma(x2, w2, a2[c]);
      }
      if (KDELAY == i + 1) { asm volatile("" :: "v"(a2[0]), "v"(a2[3])); SB(); LDSBAR(); SB(); }
    }
    STAMP(1);
    {
      const f32x2 r = {tr16(tr32(a2[0][0], a2[2][0]), tr32(a2[1][0], a2[3][0])), tr16(tr32(a2[0][1], a2[2][1]), tr32(a2[1][1], a2[3][1]))};
      *(LAS f32x2*)(L + L_QP + p * 512 + (l & 15) * 32 + (l >> 5) * 16 + ((l >> 4) & 1) * 8) = r;
    }
    const u64 ep1 = (((u64)(unsigned)__builtin_amdgcn_readfirstlane((int)(epv >> 32)) << 32) | (unsigned)__builtin_amdgcn_readfirstlane((int)epv)) + 1ull;
    const u64 T = ep1 * 0x9E3779B97F4A7C15ull;
    const unsigned hi = (unsigned)(T >> 32), lo = (unsigned)T, tag = hi;
    const v4u fo = {(lo & 63u) << 4, (lo >> 6) & 1023u, (lo >> 16) & 1023u, ((lo >> 26) | (hi << 6)) & 1023u};
    asm volatile("s_waitcnt lgkmcnt(0)" :: "s"(hi), "s"(lo) : "memory");
    unsigned arr = 0;
    if (l == 0) arr = __hip_atomic_fetch_add((LAS unsigned*)(L + L_CNT), 1u, __ATOMIC_RELAXED, __HIP_MEMORY_SCOPE_WORKGROUP);
    arr = __builtin_amdgcn_readfirstlane(arr);
    if (arr == NPW - 1) {
      asm volatile("" ::: "memory");
      f32x2 q = *(volatile LAS f32x2*)(L + L_QP + l * 8);
#pragma unroll
      for (int ww = 1; ww < NPW; ++ww) q += *(volatile LAS f32x2*)(L + L_QP + ww * 512 + l * 8);
      __hip_atomic_store((GAS u64*)(ws + WS_QG) + me * 64 + l, ((u64)tag << 32) | cvtpk(q[0], q[1]), __ATOMIC_RELAXED, __HIP_MEMORY_SCOPE_AGENT);
    }
    STAMP(3);
    if (p == 3 && l < 2) *(LAS v4u*)(L + L_TAG + l * 16) = l == 0 ? fo : v4u{(hi >> 4) & 511u, (hi >> 13) & 511u, (hi >> 22) & 511u, ((hi >> 31) | (lo << 1)) & 511u};
    const bool lead = g == 0 && s == 1;
    int eo = OOB; unsigned e0, e1;
    {
      const int qo = WS_QG + (grp * 16 + 4 * p) * 512 + l * 16;
      v4u qa0, qa1, qb0, qb1, r0, r1;
      auto sig = [&](unsigned m) {
        __builtin_amdgcn_s_setprio(0);
        for (unsigned spins = 0; !(__hip_atomic_load((LAS unsigned*)(L + L_SIG), __ATOMIC_RELAXED, __HIP_MEMORY_SCOPE_WORKGROUP) & m) && spins < (SPIN_CAP << 4); ++spins) __builtin_amdgcn_s_sleep(2);
        __builtin_amdgcn_s_setprio(3);
      };
      auto tagged = [&](const v4u& a, const v4u& c) -> bool {
        return __builtin_amdgcn_ballot_w64(((a[1] ^ tag) | (a[3] ^ tag) | (c[1] ^ tag) | (c[3] ^ tag)) != 0u) == 0ull;
      };
      sig(1u);
      STAMP(7);
      asm volatile("" ::: "memory");
      qa0 = __builtin_amdgcn_raw_buffer_load_b128(rs, qo, 0, AUX_SC1);
      qa1 = __builtin_amdgcn_raw_buffer_load_b128(rs, qo + 1024, 0, AUX_SC1);
      SB();
      sig(2u);
      asm volatile("" ::: "memory");
      qb0 = __builtin_amdgcn_raw_buffer_load_b128(rs, qo, 0, AUX_SC1);
      qb1 = __builtin_amdgcn_raw_buffer_load_b128(rs, qo + 1024, 0, AUX_SC1);
      if (p == 3 && lead) eo = WS_QG + (b * 128 + l) * 512 + 4;
      e0 = __builtin_amdgcn_raw_buffer_load_b32(rs, eo, 0, AUX_SC1);
      e1 = __builtin_amdgcn_raw_buffer_load_b32(rs, eo, 64 * 512, AUX_SC1);
      SB();
      bool done = tagged(qa0, qa1);
      r0 = qa0; r1 = qa1;
      STAMP(9);
      if (!done) {
        done = tagged(qb0, qb1);
        r0 = qb0; r1 = qb1;
        STAMP(10);
        for (unsigned spins = 0; !done && spins < SPIN_CAP; ++spins) {
          asm volatile("" ::: "memory");
          r0 = __builtin_amdgcn_raw_buffer_load_b128(rs, qo, 0, AUX_SC1);
          r1 = __builtin_amdgcn_raw_buffer_load_b128(rs, qo + 1024, 0, AUX_SC1);
          done = tagged(r0, r1);
        }
      }
      const float a = tr32(bflo(r0[0]) + bflo(r1[0]), bflo(r0[2]) + bflo(r1[2])), c = tr32(bfhi(r0[0]) + bfhi(r1[0]), bfhi(r0[2]) + bfhi(r1[2]));
      *(LAS unsigned*)(L + L_QS + p * 256 + (l & 31) * 8 + (l >> 5) * 4) = cvtpk(a, c);
      STAMP(4);
    }
    LDSBAR();
    if (p == 3 && lead) {
      for (unsigned spins = 0; !__all(e0 == tag && e1 == tag) && spins < SPIN_CAP; ++spins) {
        asm volatile("" ::: "memory");
        e0 = __builtin_amdgcn_raw_buffer_load_b32(rs, eo, 0, AUX_SC1);
        e1 = __builtin_amdgcn_raw_buffer_load_b32(rs, eo, 64 * 512, AUX_SC1);
      }
      if (__all(e0 == tag && e1 == tag) && l == 0) __hip_atomic_store((GAS u64*)(ws + WS_EPOCH + b * 256), ep1, __ATOMIC_RELAXED, __HIP_MEMORY_SCOPE_AGENT);
      if (X_EXIT != 2 && X_EXIT != 6) { LDSBAR(); if (X_EXIT != 4) LDSBAR(); }
    } else if (p == 2 && X_EXIT != 2 && X_EXIT != 6) {
      LDSBAR();
      const int a0 = WS_OR + (grp * 16 + (l >> 3)) * 2048 + s * 128 + (l & 7) * 16;
      v4u o0, o1;
      poll2<ADELAY, 0x3F0u, 0x3FFu>(rs, a0, a0 + 8 * 2048, fo, o0, o1);
      STAMP(9);
      unsigned ea = (o0[0] & 15u) << ((l & 7) * 4), eb = (o1[0] & 15u) << ((l & 7) * 4);
      ea |= dppu<XOR1>(ea); eb |= dppu<XOR1>(eb);
      ea |= dppu<XOR2>(ea); eb |= dppu<XOR2>(eb);
      ea |= dppu<XOR7>(ea); eb |= dppu<XOR7>(eb);
      const float ma = __uint_as_float(ea), mb = __uint_as_float(eb);
      float Mg = fmx(ma, mb);
      Mg = x16_32_max(fmx(Mg, dpp<ROR8>(Mg)));
      const float wa = __builtin_amdgcn_exp2f(ma - Mg), wb = __builtin_amdgcn_exp2f(mb - Mg);
      float ov[4];
#pragma unroll
      for (int i = 0; i < 4; ++i) ov[i] = fmaf(wa, __uint_as_float(o0[i] & ~0x3FFu), wb * __uint_as_float(o1[i] & ~0x3FFu));
      float Lg = wa + wb;
      Lg += dpp<ROR8>(Lg);
      Lg = tr16(Lg, Lg); Lg = tr32(Lg, Lg);
      const float o = tr_reduce4(ov[0], ov[1], ov[2], ov[3]) * __builtin_amdgcn_rcpf(Lg);
      if (!(l & 8)) ((LAS float*)(L + L_OV))[(l & 7) * 4 + (l >> 5) * 2 + ((l >> 4) & 1)] = o;
      LDSBAR();
    }
    STAMP_FLUSH();
    return;
  }

  const int w = wv - NPW;
  const __amdgpu_buffer_rsrc_t rk = mkrs(b ? kB : kA, HKV * SK * 256);
  const __amdgpu_buffer_rsrc_t rv = mkrs(b ? vB : vA, HKV * SK * 256);
  const __amdgpu_buffer_rsrc_t ro = mkrs(b ? woB : woA, 4096 * 1024);
  const int kvbase = (g * SK + (SK - W) + s * 256 + w * 32) * 256;
  const int rot = (w + s) & 7;
  const int kvo = ((l & 15) >> 2) * 256 + (l >> 4) * 64 + (l & 3) * 16;
  __builtin_amdgcn_s_setprio(2);
  LDSBAR();
  SB();
  v4u kf[8], vf[8];
#pragma unroll
  for (int m = 0; m < 8; ++m) kf[m] = bld<AUX_NT>(rk, kvo, kvbase + (m ^ rot) * 1024);
  SB();
  __builtin_amdgcn_s_setprio(1);
#pragma unroll
  for (int m = 0; m < 8; ++m) {
    vf[m] = bld<AUX_NT>(rv, l * 16, kvbase + (m ^ rot) * 1024);
    SB();
    if (m + 1 == POS1 || m + 1 == POS2) {
      if (l == 0 && ((m + 1 == POS1 && w == 0) || (m + 1 == POS2 && w == 4)))
        __hip_atomic_fetch_or((LAS unsigned*)(L + L_SIG), w == 0 ? 1u : 2u, __ATOMIC_RELAXED, __HIP_MEMORY_SCOPE_WORKGROUP);
      SB();
    }
  }
  ASTAMP(1);
  LDSBAR();
  ASTAMP(2);
  const v4u qf = *(LAS v4u*)(L + L_QS + kvo);
  float sc[8];
#pragma unroll
  for (int m = 0; m < 8; ++m) {
    const f32x4 d = mfma(kf[m], qf, f32x4{0.f, 0.f, 0.f, 0.f});
    sc[m] = dpp<0xFF>(d[3]) + (dpp<0xAA>(d[2]) + (dpp<0x55>(d[1]) + dpp<0x00>(d[0])));
  }
  constexpr float C2 = 0.088388347648318440f * 1.4426950408889634f;
  const float Mc = x16_32_max(fmx(fmx3(fmx3(sc[0], sc[1], sc[2]), sc[3], sc[4]), fmx3(sc[5], sc[6], sc[7]))) * C2;
  if (l < 16 && (l & 3) == 0) ((LAS float*)(L + L_MX))[(l >> 2) * 8 + w] = Mc;
  v4u pf;
  {
    float p[8];
#pragma unroll
    for (int m = 0; m < 8; ++m) p[m] = __builtin_amdgcn_exp2f(fmaf(sc[m], C2, -Mc));
    pf = v4u{cvtpk(p[0], p[1]), cvtpk(p[2], p[3]), cvtpk(p[4], p[5]), cvtpk(p[6], p[7])};
  }
  const v4u ones = {0x3F803F80u, 0x3F803F80u, 0x3F803F80u, 0x3F803F80u};
  const f32x4 lsum = mfma(pf, ones, f32x4{0.f, 0.f, 0.f, 0.f});
  if ((l & 15) == 0) ((LAS float*)(L + L_LS))[(l >> 4) * 8 + w] = lsum[0];
  asm volatile("" ::: "memory");
  if (l == 0) __hip_atomic_fetch_add((LAS unsigned*)(L + L_MLC), 1u, __ATOMIC_RELAXED, __HIP_MEMORY_SCOPE_WORKGROUP);
  ASTAMP(3);
  SB();
  v4u wo[4];
  {
    __builtin_amdgcn_s_setprio(0);
    const int vo = (l >> 3) * 4 * 1024 + (l & 7) * 16, so = (g * 512 + s * 32) * 1024 + w * 128;
#pragma unroll
    for (int i = 0; i < 4; ++i) wo[i] = bld<AUX_NT>(ro, vo + i * 1024, so);
    SB();
  }
  {
    v4u bfr[8];
#pragma unroll
    for (int u = 0; u < 4; ++u) {
#pragma unroll
      for (int d = 0; d < 8; ++d) bfr[d][u] = __builtin_amdgcn_perm(vf[2 * u + 1][d >> 1], vf[2 * u][d >> 1], (d & 1) ? 0x07060302u : 0x05040100u);
      if (u < 3) SB();
    }
    float o[8];
#pragma unroll
    for (int d = 0; d < 8; ++d) o[d] = mfma(pf, bfr[d], f32x4{0.f, 0.f, 0.f, 0.f})[0];
    LAS f32x4* om = (LAS f32x4*)(L + L_OM + w * 2048 + l * 16);
    om[0] = f32x4{o[0], o[1], o[2], o[3]};
    om[64] = f32x4{o[4], o[5], o[6], o[7]};
  }
  float ew[8]; v4u fw = {0u, 0u, 0u, 0u};
  if (w < 2) {
    for (unsigned spins = 0; __hip_atomic_load((LAS unsigned*)(L + L_MLC), __ATOMIC_RELAXED, __HIP_MEMORY_SCOPE_WORKGROUP) != 8u && spins < (SPIN_CAP << 6); ++spins) { }
    asm volatile("" ::: "memory");
    const int h = (w * 64 + l) >> 5;
    const f32x4 m0 = *(LAS f32x4*)(L + L_MX + h * 32), m1 = *(LAS f32x4*)(L + L_MX + h * 32 + 16);
    const f32x4 l0 = *(LAS f32x4*)(L + L_LS + h * 32), l1 = *(LAS f32x4*)(L + L_LS + h * 32 + 16);
    fw = *(LAS v4u*)(L + L_TAG);
    const float Mh = fmx(fmx3(fmx3(m0[0], m0[1], m0[2]), m0[3], m1[0]), fmx3(m1[1], m1[2], m1[3]));
    float Lh = 0.f;
#pragma unroll
    for (int ww = 0; ww < 8; ++ww) { ew[ww] = __builtin_amdgcn_exp2f((ww < 4 ? m0[ww & 3] : m1[ww & 3]) - Mh); Lh = fmaf(ew[ww], ww < 4 ? l0[ww & 3] : l1[ww & 3], Lh); }
    const float inv = __builtin_amdgcn_rcpf(Lh);
#pragma unroll
    for (int ww = 0; ww < 8; ++ww) { ew[ww] *= inv; asm volatile("" : "+v"(ew[ww])); }
    fw[0] |= (__float_as_uint(Mh + __builtin_amdgcn_logf(Lh)) >> ((l & 7) * 4)) & 15u;
    asm volatile("" : "+v"(fw));
  }
  if (X_EXIT == 2) { asm volatile("" :: "v"(wo[0]), "v"(wo[1]), "v"(wo[2]), "v"(wo[3])); return; }
  if (X_EXIT == 6) return;
  ASTAMP(6);
  LDSBAR();
  ASTAMP(7);
  if (w < 2) {
    const int t = w * 64 + l;
    const int lo = (t >> 5) * 16 + ((t & 31) >> 1), h2 = t & 1;
    f32x4 a = *(LAS f32x4*)(L + L_OM + h2 * 1024 + lo * 16) * ew[0];
#pragma unroll
    for (int ww = 1; ww < 8; ++ww) a += *(LAS f32x4*)(L + L_OM + ww * 2048 + h2 * 1024 + lo * 16) * ew[ww];
    v4u o4;
#pragma unroll
    for (int i = 0; i < 4; ++i) o4[i] = ((__float_as_uint(a[i]) + 0x200u) & ~0x3FFu) | fw[i];
    __builtin_amdgcn_raw_buffer_store_b128(o4, rs, WS_OR + me * 2048 + t * 16, 0, AUX_SC1);
    ASTAMP(8);
  }
  if (X_EXIT == 4) return;
  f32x2 wf[4][4];
#pragma unroll
  for (int i = 0; i < 4; ++i)
#pragma unroll
    for (int c = 0; c < 4; ++c) { wf[i][c] = f32x2{bflo(wo[i][c]), bfhi(wo[i][c])}; asm volatile("" : "+v"(wf[i][c])); }
  v4u fg = *(LAS v4u*)(L + L_TAG + 16);
  unsigned gs = ((l >> 3) & 2) ? (((l >> 3) & 1) ? fg[3] : fg[2]) : (((l >> 3) & 1) ? fg[1] : fg[0]);
  asm volatile("" : "+v"(fg), "+v"(gs));
  SB();
  LDSBAR();
  ASTAMP(10);
  {
    const f32x4 ov = *(LAS f32x4*)(L + L_OV + (l >> 3) * 16);
    f32x2 a2[4];
#pragma unroll
    for (int i = 0; i < 4; ++i) {
      const f32x2 o2 = {ov[i], ov[i]};
#pragma unroll
      for (int c = 0; c < 4; ++c) a2[c] = i == 0 ? o2 * wf[i][c] : __builtin_elementwise_fma(o2, wf[i][c], a2[c]);
    }
    const float acc[8] = {a2[0][0], a2[0][1], a2[1][0], a2[1][1], a2[2][0], a2[2][1], a2[3][0], a2[3][1]};
    const float res = tr_reduce8(acc, l);
    const unsigned word = ((__float_as_uint(res) + 0x100u) & ~0x1FFu) | gs;
    const unsigned colv = (unsigned)__builtin_amdgcn_ds_bpermute(((l >> 3) + ((l & 7) << 3)) * 4, (int)word);
    __builtin_amdgcn_raw_buffer_store_b32(colv, rs, WS_IN + (b * 16 + w * 2 + (l >> 5)) * 16384 + (g * 16 + s) * 128 + (l & 31) * 4, 0, AUX_SC1);
  }
  if (X_EXIT == 5) return;
  ASTAMP(11);
  if ((s & 7) == 0) {
    const int cidx = b * 16 + g * 2 + (s >> 3);
    const int ai = WS_IN + cidx * 16384 + w * 2048 + l * 16;
    v4u v0, v1;
    poll2<BDELAY, 0x1FFu, 0x1FFu>(rs, ai, ai + 1024, fg, v0, v1);
    ASTAMP(12);
    float sv[4];
#pragma unroll
    for (int i = 0; i < 4; ++i) sv[i] = __uint_as_float(v0[i] & ~0x1FFu) + __uint_as_float(v1[i] & ~0x1FFu);
    const float r = tr_reduce4(sv[0], sv[1], sv[2], sv[3]);
    if (!(l & 8)) ((LAS float*)(L + L_FIN))[w * 32 + (l & 7) * 4 + (l >> 5) * 2 + ((l >> 4) & 1)] = r;
    LDSBAR();
    ASTAMP(13);
    if (w == 0) {
      float t = 0.f;
#pragma unroll
      for (int ww = 0; ww < 8; ++ww) t += ((LAS float*)(L + L_FIN))[ww * 32 + (l & 31)];
      const float t1 = dpp<XOR1>(t);
      if (l < 32 && !(l & 1)) *(GAS unsigned*)((GAS unsigned char*)out + (cidx * 32 + l) * 2) = cvtpk(t, t1);
    }
    ASTAMP(14);
  }
  STAMP_FLUSH();
}

extern "C" void kernel_launch(void* const* d_in, const int* in_sizes, int n_in,
                              void* d_out, int out_size, void* d_ws, size_t ws_size,
                              hipStream_t stream) {
  (void)in_sizes; (void)n_in; (void)out_size; (void)ws_size;
  (void)hipFuncSetAttribute((const void*)fused_decode, hipFuncAttributeMaxDynamicSharedMemorySize, LDS_BYTES);
  typedef const unsigned char* P;
  hipLaunchKernelGGL(fused_decode, dim3(NWG), dim3(NTHR), LDS_BYTES, stream,
                     (P)d_in[0], (P)d_in[1], (P)d_in[2], (P)d_in[3], (P)d_in[4],
                     (P)d_in[5], (P)d_in[6], (P)d_in[7], (P)d_in[8], (P)d_in[9],
                     (unsigned char*)d_out, (unsigned char*)d_ws);
}
```

```cpp
#include <hip/hip_runtime.h>
#include <hip/hip_bf16.h>
#include <stdint.h>

#define GAS __attribute__((address_space(1)))
#define LAS __attribute__((address_space(3)))
typedef unsigned v4u __attribute__((ext_vector_type(4)));
typedef unsigned v2u __attribute__((ext_vector_type(2)));
typedef float f32x4 __attribute__((ext_vector_type(4)));
typedef float f32x2 __attribute__((ext_vector_type(2)));
typedef short bf16x8 __attribute__((ext_vector_type(8)));
typedef __bf16 bf16x2 __attribute__((ext_vector_type(2)));
typedef unsigned long long u64;

#define STAMPS 0
#define X_EXIT 0
#define KDELAY 1
#define POS1 6
#define POS2 3
#define ADELAY 0
#define AACK 1
#define BACK 0
#define BDELAY 0

constexpr int HKV = 8, SK = 32768, W = 4096;
constexpr int NWG = 256, NPW = 4, NTHR = (NPW + 8) * 64;
constexpr unsigned SPIN_CAP = 1u << 16;
constexpr int AUX_SC1 = 16, AUX_NT = 2, OOB = 0x7FFF0000;

constexpr int WS_EPOCH = 0;
constexpr int WS_QG = 1024;
constexpr int WS_OR = WS_QG + NWG * 512;
constexpr int WS_IN = WS_OR + NWG * 2048;
constexpr int WS_END = WS_IN + 32 * 16384;
constexpr int WS_STAMP = 2 << 20;
static_assert(WS_OR % 128 == 0 && WS_IN % 128 == 0 && WS_END <= WS_STAMP, "line-aligned records");

constexpr int L_QS = 1024;
constexpr int L_MX = 2048;
constexpr int L_LS = 2176;
constexpr int L_CNT = 2304, L_PUB = 2308, L_SIG = 2312, L_MLC = 2316;
constexpr int L_TAG = 2368;
constexpr int L_OV = 2432;
constexpr int L_FIN = 2560;
constexpr int L_OM = 4096;
constexpr int L_QP = 20480;
constexpr int LDS_BYTES = 96 * 1024;

__device__ __forceinline__ float bflo(unsigned u) { return __builtin_bit_cast(float, u << 16); }
__device__ __forceinline__ float bfhi(unsigned u) { return __builtin_bit_cast(float, u & 0xffff0000u); }
__device__ __forceinline__ unsigned cvtpk(float lo, float hi) {
  const f32x2 v = {lo, hi};
  return __builtin_bit_cast(unsigned, __builtin_convertvector(v, bf16x2));
}
template <int CTRL> __device__ __forceinline__ float dpp(float x) {
  return __builtin_bit_cast(float, __builtin_amdgcn_mov_dpp(__builtin_bit_cast(int, x), CTRL, 0xf, 0xf, true));
}
template <int CTRL> __device__ __forceinline__ unsigned dppu(unsigned x) { return (unsigned)__builtin_amdgcn_mov_dpp((int)x, CTRL, 0xf, 0xf, true); }
constexpr int XOR1 = 0xB1, XOR2 = 0x4E, XOR7 = 0x141, ROR8 = 0x128;
__device__ __forceinline__ float fmx(float a, float b) { return __builtin_fmaxf(__builtin_fmaxf(a, b), -__builtin_inff()); }
__device__ __forceinline__ float fmx3(float a, float b, float c) { return __builtin_fmaxf(__builtin_fmaxf(a, b), c); }
__device__ __forceinline__ float tr32(float a, float b) { auto t = __builtin_amdgcn_permlane32_swap(__float_as_uint(a), __float_as_uint(b), false, false); return __uint_as_float(t[0]) + __uint_as_float(t[1]); }
__device__ __forceinline__ float tr16(float a, float b) { auto t = __builtin_amdgcn_permlane16_swap(__float_as_uint(a), __float_as_uint(b), false, false); return __uint_as_float(t[0]) + __uint_as_float(t[1]); }
__device__ __forceinline__ float x16_32_max(float x) {
  auto s = __builtin_amdgcn_permlane16_swap(__float_as_uint(x), __float_as_uint(x), false, false);
  x = fmx(__uint_as_float(s[0]), __uint_as_float(s[1]));
  auto t = __builtin_amdgcn_permlane32_swap(__float_as_uint(x), __float_as_uint(x), false, false);
  return fmx(__uint_as_float(t[0]), __uint_as_float(t[1]));
}
__device__ __forceinline__ float tr_reduce8(const float (&a)[8], int l) {
  const float r0 = tr16(tr32(a[0], a[4]), tr32(a[2], a[6])), r1 = tr16(tr32(a[1], a[5]), tr32(a[3], a[7]));
  const float u0 = r0 + dpp<ROR8>(r0), u1 = r1 + dpp<ROR8>(r1);
  return (l & 8) ? u1 : u0;
}
__device__ __forceinline__ float tr_reduce4(float a0, float a1, float a2, float a3) {
  const float r = tr16(tr32(a0, a2), tr32(a1, a3));
  return r + dpp<ROR8>(r);
}
__device__ __forceinline__ f32x4 mfma(v4u a, v4u b, f32x4 c) {
  return __builtin_amdgcn_mfma_f32_16x16x32_bf16(__builtin_bit_cast(bf16x8, a), __builtin_bit_cast(bf16x8, b), c, 0, 0, 0);
}
template <unsigned M0, unsigned M> __device__ __forceinline__ unsigned tagdiff(const v4u& a, const v4u& f) {
  return (((a[0] ^ f[0]) & M0) | ((a[1] ^ f[1]) & M)) | (((a[2] ^ f[2]) & M) | ((a[3] ^ f[3]) & M));
}
#define LDSBAR() do { asm volatile("s_waitcnt lgkmcnt(0)" ::: "memory"); __builtin_amdgcn_s_barrier(); asm volatile("" ::: "memory"); } while (0)
#define SB() __builtin_amdgcn_sched_barrier(0)
template <int DELAY, unsigned M0, unsigned M>
__device__ __forceinline__ void poll2(__amdgpu_buffer_rsrc_t rs, int a0, int a1, const v4u& f, v4u& o0, v4u& o1) {
  if (DELAY) { __builtin_amdgcn_s_sleep(DELAY); SB(); }
  for (unsigned spins = 0;; ++spins) {
    asm volatile("" ::: "memory");
    o0 = __builtin_amdgcn_raw_buffer_load_b128(rs, a0, 0, AUX_SC1);
    o1 = __builtin_amdgcn_raw_buffer_load_b128(rs, a1, 0, AUX_SC1);
    if (__builtin_amdgcn_ballot_w64((tagdiff<M0, M>(o0, f) | tagdiff<M0, M>(o1, f)) != 0u) == 0ull || spins >= SPIN_CAP) return;
  }
}
#define ASTAMP(i) do { } while (0)
#define STAMP(i) do { } while (0)
#define STAMP_FLUSH() do { } while (0)
__device__ __forceinline__ __amdgpu_buffer_rsrc_t mkrs(const void* p, int bytes) { return __builtin_amdgcn_make_buffer_rsrc((void*)p, 0, bytes, 0x00020000); }
template <int AUX> __device__ __forceinline__ v4u bld(__amdgpu_buffer_rsrc_t r, int voff, int soff) { return __builtin_amdgcn_raw_buffer_load_b128(r, voff, soff, AUX); }

__global__ void __launch_bounds__(NTHR, 3)
fused_decode(const unsigned char* __restrict__ xA, const unsigned char* __restrict__ kA, const unsigned char* __restrict__ vA,
             const unsigned char* __restrict__ wqA, const unsigned char* __restrict__ woA,
             const unsigned char* __restrict__ xB, const unsigned char* __restrict__ kB, const unsigned char* __restrict__ vB,
             const unsigned char* __restrict__ wqB, const unsigned char* __restrict__ woB,
             unsigned char* __restrict__ out, unsigned char* ws_) {
  asm volatile("" :: "s"(xA), "s"(kA), "s"(vA), "s"(wqA), "s"(woA), "s"(xB), "s"(kB), "s"(vB), "s"(wqB), "s"(woB), "s"(out), "s"(ws_));
  STAMP(15);
  extern __shared__ __attribute__((aligned(16))) unsigned char smem[];
  LAS unsigned char* L = (LAS unsigned char*)smem;
  GAS unsigned char* ws = (GAS unsigned char*)ws_;
  const int tid = threadIdx.x, l = tid & 63;
  const int wv = __builtin_amdgcn_readfirstlane(tid >> 6);
  const int bid = blockIdx.x, g = bid & 7, j = bid >> 3, b = j >> 4, s = j & 15;
  const int grp = b * 8 + g, me = grp * 16 + s;
  const __amdgpu_buffer_rsrc_t rs = mkrs(ws_, WS_END);
  if (X_EXIT >= 100) { const u64 t0_ = __builtin_amdgcn_s_memrealtime(); while (__builtin_amdgcn_s_memrealtime() - t0_ < (u64)(X_EXIT)) __builtin_amdgcn_s_sleep(1); return; }

  if (__builtin_expect(wv < NPW, 1)) {
    __builtin_amdgcn_s_setprio(3);
    const int p = wv;
    const __amdgpu_buffer_rsrc_t rx = mkrs(b ? xB : xA, 1024);
    const __amdgpu_buffer_rsrc_t rq = mkrs(b ? wqB : wqA, 512 * 8192);
    const v4u xv = bld<0>(rx, (l >> 4) * 16, (s & 3) * 256 + p * 64);
    SB();
    v4u wq[8];
    {
      const int vo = (l >> 4) * 8 * 8192 + (l & 15) * 16, so = ((s & 3) * 128 + p * 32) * 8192 + (g * 512 + (s >> 2) * 128) * 2;
#pragma unroll
      for (int i = 0; i < 8; ++i) wq[i] = bld<AUX_NT>(rq, vo, so + i * 8192);
    }
    SB();
    STAMP(16);
    if (tid == 0) *(LAS v4u*)(L + L_CNT) = v4u{0u, 0u, 0u, 0u};
    const u64 epv = __hip_atomic_load((GAS u64*)(ws + WS_EPOCH + b * 256), __ATOMIC_RELAXED, __HIP_MEMORY_SCOPE_AGENT);
    SB();
    if (KDELAY == 0) { LDSBAR(); SB(); }
    f32x2 a2[4];
#pragma unroll
    for (int i = 0; i < 8; ++i) {
      const float xs = (i & 1) ? bfhi(xv[i >> 1]) : bflo(xv[i >> 1]);
      const f32x2 x2 = {xs, xs};
#pragma unroll
      for (int c = 0; c < 4; ++c) {
        const f32x2 w2 = {bflo(wq[i][c]), bfhi(wq[i][c])};
        a2[c] = i == 0 ? x2 * w2 : __builtin_elementwise_fma(x2, w2, a2[c]);
      }
      if (KDELAY == i + 1) { asm volatile("" :: "v"(a2[0]), "v"(a2[3])); SB(); LDSBAR(); SB(); }
    }
    STAMP(1);
    {
      const f32x2 r = {tr16(tr32(a2[0][0], a2[2][0]), tr32(a2[1][0], a2[3][0])), tr16(tr32(a2[0][1], a2[2][1]), tr32(a2[1][1], a2[3][1]))};
      *(LAS f32x2*)(L + L_QP + p * 512 + (l & 15) * 32 + (l >> 5) * 16 + ((l >> 4) & 1) * 8) = r;
    }
    const u64 ep1 = (((u64)(unsigned)__builtin_amdgcn_readfirstlane((int)(epv >> 32)) << 32) | (unsigned)__builtin_amdgcn_readfirstlane((int)epv)) + 1ull;
    const u64 T = ep1 * 0x9E3779B97F4A7C15ull;
    const unsigned hi = (unsigned)(T >> 32), lo = (unsigned)T, tag = hi;
    const v4u fo = {(lo & 63u) << 4, (lo >> 6) & 1023u, (lo >> 16) & 1023u, ((lo >> 26) | (hi << 6)) & 1023u};
    asm volatile("s_waitcnt lgkmcnt(0)" :: "s"(hi), "s"(lo) : "memory");
    unsigned arr = 0;
    if (l == 0) arr = __hip_atomic_fetch_add((LAS unsigned*)(L + L_CNT), 1u, __ATOMIC_RELAXED, __HIP_MEMORY_SCOPE_WORKGROUP);
    arr = __builtin_amdgcn_readfirstlane(arr);
    if (arr == NPW - 1) {
      asm volatile("" ::: "memory");
      f32x2 q = *(volatile LAS f32x2*)(L + L_QP + l * 8);
#pragma unroll
      for (int ww = 1; ww < NPW; ++ww) q += *(volatile LAS f32x2*)(L + L_QP + ww * 512 + l * 8);
      __hip_atomic_store((GAS u64*)(ws + WS_QG) + me * 64 + l, ((u64)tag << 32) | cvtpk(q[0], q[1]), __ATOMIC_RELAXED, __HIP_MEMORY_SCOPE_AGENT);
    }
    STAMP(3);
    if (p == 3 && l < 2) *(LAS v4u*)(L + L_TAG + l * 16) = l == 0 ? fo : v4u{(hi >> 4) & 511u, (hi >> 13) & 511u, (hi >> 22) & 511u, ((hi >> 31) | (lo << 1)) & 511u};
    const bool lead = g == 0 && s == 1;
    int eo = OOB; unsigned e0, e1;
    {
      const int qo = WS_QG + (grp * 16 + 4 * p) * 512 + l * 16;
      v4u qa0, qa1, qb0, qb1, r0, r1;
      auto sig = [&](unsigned m) {
        __builtin_amdgcn_s_setprio(0);
        for (unsigned spins = 0; !(__hip_atomic_load((LAS unsigned*)(L + L_SIG), __ATOMIC_RELAXED, __HIP_MEMORY_SCOPE_WORKGROUP) & m) && spins < (SPIN_CAP << 4); ++spins) __builtin_amdgcn_s_sleep(2);
        __builtin_amdgcn_s_setprio(3);
      };
      auto tagged = [&](const v4u& a, const v4u& c) -> bool {
        return __builtin_amdgcn_ballot_w64(((a[1] ^ tag) | (a[3] ^ tag) | (c[1] ^ tag) | (c[3] ^ tag)) != 0u) == 0ull;
      };
      sig(1u);
      STAMP(7);
      asm volatile("" ::: "memory");
      qa0 = __builtin_amdgcn_raw_buffer_load_b128(rs, qo, 0, AUX_SC1);
      qa1 = __builtin_amdgcn_raw_buffer_load_b128(rs, qo + 1024, 0, AUX_SC1);
      SB();
      sig(2u);
      asm volatile("" ::: "memory");
      qb0 = __builtin_amdgcn_raw_buffer_load_b128(rs, qo, 0, AUX_SC1);
      qb1 = __builtin_amdgcn_raw_buffer_load_b128(rs, qo + 1024, 0, AUX_SC1);
      if (p == 3 && lead) eo = WS_QG + (b * 128 + l) * 512 + 4;
      e0 = __builtin_amdgcn_raw_buffer_load_b32(rs, eo, 0, AUX_SC1);
      e1 = __builtin_amdgcn_raw_buffer_load_b32(rs, eo, 64 * 512, AUX_SC1);
      SB();
      bool done = tagged(qa0, qa1);
      r0 = qa0; r1 = qa1;
      STAMP(9);
      if (!done) {
        done = tagged(qb0, qb1);
        r0 = qb0; r1 = qb1;
        STAMP(10);
        for (unsigned spins = 0; !done && spins < SPIN_CAP; ++spins) {
          asm volatile("" ::: "memory");
          r0 = __builtin_amdgcn_raw_buffer_load_b128(rs, qo, 0, AUX_SC1);
          r1 = __builtin_amdgcn_raw_buffer_load_b128(rs, qo + 1024, 0, AUX_SC1);
          done = tagged(r0, r1);
        }
      }
      const float a = tr32(bflo(r0[0]) + bflo(r1[0]), bflo(r0[2]) + bflo(r1[2])), c = tr32(bfhi(r0[0]) + bfhi(r1[0]), bfhi(r0[2]) + bfhi(r1[2]));
      *(LAS unsigned*)(L + L_QS + p * 256 + (l & 31) * 8 + (l >> 5) * 4) = cvtpk(a, c);
      STAMP(4);
    }
    LDSBAR();
    if (p == 3 && lead) {
      for (unsigned spins = 0; !__all(e0 == tag && e1 == tag) && spins < SPIN_CAP; ++spins) {
        asm volatile("" ::: "memory");
        e0 = __builtin_amdgcn_raw_buffer_load_b32(rs, eo, 0, AUX_SC1);
        e1 = __builtin_amdgcn_raw_buffer_load_b32(rs, eo, 64 * 512, AUX_SC1);
      }
      if (__all(e0 == tag && e1 == tag) && l == 0) __hip_atomic_store((GAS u64*)(ws + WS_EPOCH + b * 256), ep1, __ATOMIC_RELAXED, __HIP_MEMORY_SCOPE_AGENT);
      if (X_EXIT != 2 && X_EXIT != 6) { LDSBAR(); if (X_EXIT != 4) LDSBAR(); }
    } else if (p == 2 && X_EXIT != 2 && X_EXIT != 6) {
      LDSBAR();
      const int a0 = WS_OR + (grp * 16 + (l >> 3)) * 2048 + s * 128 + (l & 7) * 16;
      v4u o0, o1;
      if (AACK) for (unsigned spins = 0; __hip_atomic_load((LAS unsigned*)(L + L_PUB), __ATOMIC_RELAXED, __HIP_MEMORY_SCOPE_WORKGROUP) != 2u && spins < (SPIN_CAP << 6); ++spins) __builtin_amdgcn_s_sleep(1);
      poll2<ADELAY, 0x3F0u, 0x3FFu>(rs, a0, a0 + 8 * 2048, fo, o0, o1);
      STAMP(9);
      unsigned ea = (o0[0] & 15u) << ((l & 7) * 4), eb = (o1[0] & 15u) << ((l & 7) * 4);
      ea |= dppu<XOR1>(ea); eb |= dppu<XOR1>(eb);
      ea |= dppu<XOR2>(ea); eb |= dppu<XOR2>(eb);
      ea |= dppu<XOR7>(ea); eb |= dppu<XOR7>(eb);
      const float ma = __uint_as_float(ea), mb = __uint_as_float(eb);
      float Mg = fmx(ma, mb);
      Mg = x16_32_max(fmx(Mg, dpp<ROR8>(Mg)));
      const float wa = __builtin_amdgcn_exp2f(ma - Mg), wb = __builtin_amdgcn_exp2f(mb - Mg);
      float ov[4];
#pragma unroll
      for (int i = 0; i < 4; ++i) ov[i] = fmaf(wa, __uint_as_float(o0[i] & ~0x3FFu), wb * __uint_as_float(o1[i] & ~0x3FFu));
      float Lg = wa + wb;
      Lg += dpp<ROR8>(Lg);
      Lg = tr16(Lg, Lg); Lg = tr32(Lg, Lg);
      const float o = tr_reduce4(ov[0], ov[1], ov[2], ov[3]) * __builtin_amdgcn_rcpf(Lg);
      if (!(l & 8)) ((LAS float*)(L + L_OV))[(l & 7) * 4 + (l >> 5) * 2 + ((l >> 4) & 1)] = o;
      LDSBAR();
    }
    STAMP_FLUSH();
    return;
  }

  const int w = wv - NPW;
  const __amdgpu_buffer_rsrc_t rk = mkrs(b ? kB : kA, HKV * SK * 256);
  const __amdgpu_buffer_rsrc_t rv = mkrs(b ? vB : vA, HKV * SK * 256);
  const __amdgpu_buffer_rsrc_t ro = mkrs(b ? woB : woA, 4096 * 1024);
  const int kvbase = (g * SK + (SK - W) + s * 256 + w * 32) * 256;
  const int rot = (w + s) & 7;
  const int kvo = ((l & 15) >> 2) * 256 + (l >> 4) * 64 + (l & 3) * 16;
  __builtin_amdgcn_s_setprio(2);
  LDSBAR();
  SB();
  v4u kf[8], vf[8];
#pragma unroll
  for (int m = 0; m < 8; ++m) kf[m] = bld<AUX_NT>(rk, kvo, kvbase + (m ^ rot) * 1024);
  SB();
  __builtin_amdgcn_s_setprio(1);
#pragma unroll
  for (int m = 0; m < 8; ++m) {
    vf[m] = bld<AUX_NT>(rv, l * 16, kvbase + (m ^ rot) * 1024);
    SB();
    if (m + 1 == POS1 || m + 1 == POS2) {
      if (l == 0 && ((m + 1 == POS1 && w == 0) || (m + 1 == POS2 && w == 4)))
        __hip_atomic_fetch_or((LAS unsigned*)(L + L_SIG), w == 0 ? 1u : 2u, __ATOMIC_RELAXED, __HIP_MEMORY_SCOPE_WORKGROUP);
      SB();
    }
  }
  ASTAMP(1);
  LDSBAR();
  ASTAMP(2);
  const v4u qf = *(LAS v4u*)(L + L_QS + kvo);
  float sc[8];
#pragma unroll
  for (int m = 0; m < 8; ++m) {
    const f32x4 d = mfma(kf[m], qf, f32x4{0.f, 0.f, 0.f, 0.f});
    sc[m] = dpp<0xFF>(d[3]) + (dpp<0xAA>(d[2]) + (dpp<0x55>(d[1]) + dpp<0x00>(d[0])));
  }
  constexpr float C2 = 0.088388347648318440f * 1.4426950408889634f;
  const float Mc = x16_32_max(fmx(fmx3(fmx3(sc[0], sc[1], sc[2]), sc[3], sc[4]), fmx3(sc[5], sc[6], sc[7]))) * C2;
  if (l < 16 && (l & 3) == 0) ((LAS float*)(L + L_MX))[(l >> 2) * 8 + w] = Mc;
  v4u pf;
  {
    float p[8];
#pragma unroll
    for (int m = 0; m < 8; ++m) p[m] = __builtin_amdgcn_exp2f(fmaf(sc[m], C2, -Mc));
    pf = v4u{cvtpk(p[0], p[1]), cvtpk(p[2], p[3]), cvtpk(p[4], p[5]), cvtpk(p[6], p[7])};
  }
  const v4u ones = {0x3F803F80u, 0x3F803F80u, 0x3F803F80u, 0x3F803F80u};
  const f32x4 lsum = mfma(pf, ones, f32x4{0.f, 0.f, 0.f, 0.f});
  if ((l & 15) == 0) ((LAS float*)(L + L_LS))[(l >> 4) * 8 + w] = lsum[0];
  asm volatile("" ::: "memory");
  if (l == 0) __hip_atomic_fetch_add((LAS unsigned*)(L + L_MLC), 1u, __ATOMIC_RELAXED, __HIP_MEMORY_SCOPE_WORKGROUP);
  ASTAMP(3);
  SB();
  v4u wo[4];
  {
    __builtin_amdgcn_s_setprio(0);
    const int vo = (l >> 3) * 4 * 1024 + (l & 7) * 16, so = (g * 512 + s * 32) * 1024 + w * 128;
#pragma unroll
    for (int i = 0; i < 4; ++i) wo[i] = bld<AUX_NT>(ro, vo + i * 1024, so);
    SB();
  }
  {
    v4u bfr[8];
#pragma unroll
    for (int u = 0; u < 4; ++u) {
#pragma unroll
      for (int d = 0; d < 8; ++d) bfr[d][u] = __builtin_amdgcn_perm(vf[2 * u + 1][d >> 1], vf[2 * u][d >> 1], (d & 1) ? 0x07060302u : 0x05040100u);
      if (u < 3) SB();
    }
    float o[8];
#pragma unroll
    for (int d = 0; d < 8; ++d) o[d] = mfma(pf, bfr[d], f32x4{0.f, 0.f, 0.f, 0.f})[0];
    LAS f32x4* om = (LAS f32x4*)(L + L_OM + w * 2048 + l * 16);
    om[0] = f32x4{o[0], o[1], o[2], o[3]};
    om[64] = f32x4{o[4], o[5], o[6], o[7]};
  }
  float ew[8]; v4u fw = {0u, 0u, 0u, 0u};
  if (w < 2) {
    for (unsigned spins = 0; __hip_atomic_load((LAS unsigned*)(L + L_MLC), __ATOMIC_RELAXED, __HIP_MEMORY_SCOPE_WORKGROUP) != 8u && spins < (SPIN_CAP << 6); ++spins) { }
    asm volatile("" ::: "memory");
    const int h = (w * 64 + l) >> 5;
    const f32x4 m0 = *(LAS f32x4*)(L + L_MX + h * 32), m1 = *(LAS f32x4*)(L + L_MX + h * 32 + 16);
    const f32x4 l0 = *(LAS f32x4*)(L + L_LS + h * 32), l1 = *(LAS f32x4*)(L + L_LS + h * 32 + 16);
    fw = *(LAS v4u*)(L + L_TAG);
    const float Mh = fmx(fmx3(fmx3(m0[0], m0[1], m0[2]), m0[3], m1[0]), fmx3(m1[1], m1[2], m1[3]));
    float Lh = 0.f;
#pragma unroll
    for (int ww = 0; ww < 8; ++ww) { ew[ww] = __builtin_amdgcn_exp2f((ww < 4 ? m0[ww & 3] : m1[ww & 3]) - Mh); Lh = fmaf(ew[ww], ww < 4 ? l0[ww & 3] : l1[ww & 3], Lh); }
    const float inv = __builtin_amdgcn_rcpf(Lh);
#pragma unroll
    for (int ww = 0; ww < 8; ++ww) { ew[ww] *= inv; asm volatile("" : "+v"(ew[ww])); }
    fw[0] |= (__float_as_uint(Mh + __builtin_amdgcn_logf(Lh)) >> ((l & 7) * 4)) & 15u;
    asm volatile("" : "+v"(fw));
  }
  if (X_EXIT == 2) { asm volatile("" :: "v"(wo[0]), "v"(wo[1]), "v"(wo[2]), "v"(wo[3])); return; }
  if (X_EXIT == 6) return;
  ASTAMP(6);
  LDSBAR();
  ASTAMP(7);
  if (w < 2) {
    const int t = w * 64 + l;
    const int lo = (t >> 5) * 16 + ((t & 31) >> 1), h2 = t & 1;
    f32x4 a = *(LAS f32x4*)(L + L_OM + h2 * 1024 + lo * 16) * ew[0];
#pragma unroll
    for (int ww = 1; ww < 8; ++ww) a += *(LAS f32x4*)(L + L_OM + ww * 2048 + h2 * 1024 + lo * 16) * ew[ww];
    v4u o4;
#pragma unroll
    for (int i = 0; i < 4; ++i) o4[i] = ((__float_as_uint(a[i]) + 0x200u) & ~0x3FFu) | fw[i];
    __builtin_amdgcn_raw_buffer_store_b128(o4, rs, WS_OR + me * 2048 + t * 16, 0, AUX_SC1);
    if (AACK) {
      asm volatile("s_waitcnt vmcnt(0)" ::: "memory");
      if (l == 0) __hip_atomic_fetch_add((LAS unsigned*)(L + L_PUB), 1u, __ATOMIC_RELAXED, __HIP_MEMORY_SCOPE_WORKGROUP);
    }
    ASTAMP(8);
  }
  if (X_EXIT == 4) return;
  f32x2 wf[4][4];
#pragma unroll
  for (int i = 0; i < 4; ++i)
#pragma unroll
    for (int c = 0; c < 4; ++c) { wf[i][c] = f32x2{bflo(wo[i][c]), bfhi(wo[i][c])}; asm volatile("" : "+v"(wf[i][c])); }
  v4u fg = *(LAS v4u*)(L + L_TAG + 16);
  unsigned gs = (l & 2) ? ((l & 1) ? fg[3] : fg[2]) : ((l & 1) ? fg[1] : fg[0]);
  asm volatile("" : "+v"(fg), "+v"(gs));
  SB();
  LDSBAR();
  ASTAMP(10);
  {
    const f32x4 ov = *(LAS f32x4*)(L + L_OV + (l >> 3) * 16);
    f32x2 a2[4];
#pragma unroll
    for (int i = 0; i < 4; ++i) {
      const f32x2 o2 = {ov[i], ov[i]};
#pragma unroll
      for (int c = 0; c < 4; ++c) a2[c] = i == 0 ? o2 * wf[i][c] : __builtin_elementwise_fma(o2, wf[i][c], a2[c]);
    }
    const float acc[8] = {a2[0][0], a2[0][1], a2[1][0], a2[1][1], a2[2][0], a2[2][1], a2[3][0], a2[3][1]};
    const float res = tr_reduce8(acc, l);
    __builtin_amdgcn_raw_buffer_store_b32(((__float_as_uint(res) + 0x100u) & ~0x1FFu) | gs,
                                          rs, WS_IN + (b * 16 + w * 2 + (l >> 5)) * 16384 + (g * 16 + s) * 128 + (l & 31) * 4, 0, AUX_SC1);
  }
  if (X_EXIT == 5) return;
  ASTAMP(11);
  if ((s & 7) == 0) {
    const int cidx = b * 16 + g * 2 + (s >> 3);
    const int ai = WS_IN + cidx * 16384 + w * 2048 + l * 16;
    v4u v0, v1;
    if (BACK) asm volatile("s_waitcnt vmcnt(0)" ::: "memory");
    poll2<BDELAY, 0x1FFu, 0x1FFu>(rs, ai, ai + 1024, fg, v0, v1);
    ASTAMP(12);
    float sv[4];
#pragma unroll
    for (int i = 0; i < 4; ++i) sv[i] = __uint_as_float(v0[i] & ~0x1FFu) + __uint_as_float(v1[i] & ~0x1FFu);
    const float r = tr_reduce4(sv[0], sv[1], sv[2], sv[3]);
    if (!(l & 8)) ((LAS float*)(L + L_FIN))[w * 32 + (l & 7) * 4 + (l >> 5) * 2 + ((l >> 4) & 1)] = r;
    LDSBAR();
    ASTAMP(13);
    if (w == 0) {
      const int pos = (l & 3) * 8 + ((l & 31) >> 2);
      float t = 0.f;
#pragma unroll
      for (int ww = 0; ww < 8; ++ww) t += ((LAS float*)(L + L_FIN))[ww * 32 + pos];
      const float t1 = dpp<XOR1>(t);
      if (l < 32 && !(l & 1)) *(GAS unsigned*)((GAS unsigned char*)out + (b * 512 + g * 64 + (s >> 3) * 4 + (l >> 2) * 8 + (l & 3)) * 2) = cvtpk(t, t1);
    }
    ASTAMP(14);
  }
  STAMP_FLUSH();
}

extern "C" void kernel_launch(void* const* d_in, const int* in_sizes, int n_in,
                              void* d_out, int out_size, void* d_ws, size_t ws_size,
                              hipStream_t stream) {
  (void)in_sizes; (void)n_in; (void)out_size; (void)ws_size;
  (void)hipFuncSetAttribute((const void*)fused_decode, hipFuncAttributeMaxDynamicSharedMemorySize, LDS_BYTES);
  typedef const unsigned char* P;
  hipLaunchKernelGGL(fused_decode, dim3(NWG), dim3(NTHR), LDS_BYTES, stream,
                     (P)d_in[0], (P)d_in[1], (P)d_in[2], (P)d_in[3], (P)d_in[4],
                     (P)d_in[5], (P)d_in[6], (P)d_in[7], (P)d_in[8], (P)d_in[9],
                     (unsigned char*)d_out, (unsigned char*)d_ws);
}
```

```cpp
#include <hip/hip_runtime.h>
#include <hip/hip_bf16.h>
#include <stdint.h>

#define GAS __attribute__((address_space(1)))
#define LAS __attribute__((address_space(3)))
typedef unsigned v4u __attribute__((ext_vector_type(4)));
typedef unsigned v2u __attribute__((ext_vector_type(2)));
typedef float f32x4 __attribute__((ext_vector_type(4)));
typedef float f32x2 __attribute__((ext_vector_type(2)));
typedef short bf16x8 __attribute__((ext_vector_type(8)));
typedef __bf16 bf16x2 __attribute__((ext_vector_type(2)));
typedef unsigned long long u64;

#define STAMPS 0
#define X_EXIT 0
#define KDELAY 2
#define POS1 4
#define POS2 2
#define ADELAY 0
#define AACK 1
#define BACK 0
#define WSMID 1
#define BDELAY 0

constexpr int HKV = 8, SK = 32768, W = 4096;
constexpr int NWG = 256, NPW = 4, NTHR = (NPW + 8) * 64;
constexpr unsigned SPIN_CAP = 1u << 16;
constexpr int AUX_SC1 = 16, AUX_NT = 2, OOB = 0x7FFF0000;

constexpr int WS_EPOCH = 0;
constexpr int WS_QG = 1024;
constexpr int WS_OR = WS_QG + NWG * 512;
constexpr int WS_IN = WS_OR + NWG * 2048;
constexpr int WS_END = WS_IN + 32 * 16384;
constexpr int WS_STAMP = 2 << 20;
static_assert(WS_OR % 128 == 0 && WS_IN % 128 == 0 && WS_END <= WS_STAMP, "line-aligned records");

constexpr int L_QS = 1024;
constexpr int L_MX = 2048;
constexpr int L_LS = 2176;
constexpr int L_CNT = 2304, L_PUB = 2308, L_SIG = 2312, L_MLC = 2316;
constexpr int L_TAG = 2368;
constexpr int L_OV = 2432;
constexpr int L_FIN = 2560;
constexpr int L_OM = 4096;
constexpr int L_QP = 20480;
constexpr int LDS_BYTES = 96 * 1024;

__device__ __forceinline__ float bflo(unsigned u) { return __builtin_bit_cast(float, u << 16); }
__device__ __forceinline__ float bfhi(unsigned u) { return __builtin_bit_cast(float, u & 0xffff0000u); }
__device__ __forceinline__ unsigned cvtpk(float lo, float hi) {
  const f32x2 v = {lo, hi};
  return __builtin_bit_cast(unsigned, __builtin_convertvector(v, bf16x2));
}
template <int CTRL> __device__ __forceinline__ float dpp(float x) {
  return __builtin_bit_cast(float, __builtin_amdgcn_mov_dpp(__builtin_bit_cast(int, x), CTRL, 0xf, 0xf, true));
}
template <int CTRL> __device__ __forceinline__ unsigned dppu(unsigned x) { return (unsigned)__builtin_amdgcn_mov_dpp((int)x, CTRL, 0xf, 0xf, true); }
constexpr int XOR1 = 0xB1, XOR2 = 0x4E, XOR7 = 0x141, ROR8 = 0x128;
__device__ __forceinline__ float fmx(float a, float b) { return __builtin_fmaxf(__builtin_fmaxf(a, b), -__builtin_inff()); }
__device__ __forceinline__ float fmx3(float a, float b, float c) { return __builtin_fmaxf(__builtin_fmaxf(a, b), c); }
__device__ __forceinline__ float tr32(float a, float b) { auto t = __builtin_amdgcn_permlane32_swap(__float_as_uint(a), __float_as_uint(b), false, false); return __uint_as_float(t[0]) + __uint_as_float(t[1]); }
__device__ __forceinline__ float tr16(float a, float b) { auto t = __builtin_amdgcn_permlane16_swap(__float_as_uint(a), __float_as_uint(b), false, false); return __uint_as_float(t[0]) + __uint_as_float(t[1]); }
__device__ __forceinline__ float x16_32_max(float x) {
  auto s = __builtin_amdgcn_permlane16_swap(__float_as_uint(x), __float_as_uint(x), false, false);
  x = fmx(__uint_as_float(s[0]), __uint_as_float(s[1]));
  auto t = __builtin_amdgcn_permlane32_swap(__float_as_uint(x), __float_as_uint(x), false, false);
  return fmx(__uint_as_float(t[0]), __uint_as_float(t[1]));
}
__device__ __forceinline__ float tr_reduce8(const float (&a)[8], int l) {
  const float r0 = tr16(tr32(a[0], a[4]), tr32(a[2], a[6])), r1 = tr16(tr32(a[1], a[5]), tr32(a[3], a[7]));
  const float u0 = r0 + dpp<ROR8>(r0), u1 = r1 + dpp<ROR8>(r1);
  return (l & 8) ? u1 : u0;
}
__device__ __forceinline__ float tr_reduce4(float a0, float a1, float a2, float a3) {
  const float r = tr16(tr32(a0, a2), tr32(a1, a3));
  return r + dpp<ROR8>(r);
}
__device__ __forceinline__ f32x4 mfma(v4u a, v4u b, f32x4 c) {
  return __builtin_amdgcn_mfma_f32_16x16x32_bf16(__builtin_bit_cast(bf16x8, a), __builtin_bit_cast(bf16x8, b), c, 0, 0, 0);
}
template <unsigned M0, unsigned M> __device__ __forceinline__ unsigned tagdiff(const v4u& a, const v4u& f) {
  return (((a[0] ^ f[0]) & M0) | ((a[1] ^ f[1]) & M)) | (((a[2] ^ f[2]) & M) | ((a[3] ^ f[3]) & M));
}
#define LDSBAR() do { asm volatile("s_waitcnt lgkmcnt(0)" ::: "memory"); __builtin_amdgcn_s_barrier(); asm volatile("" ::: "memory"); } while (0)
#define SB() __builtin_amdgcn_sched_barrier(0)
template <int DELAY, unsigned M0, unsigned M>
__device__ __forceinline__ void poll2(__amdgpu_buffer_rsrc_t rs, int a0, int a1, const v4u& f, v4u& o0, v4u& o1) {
  if (DELAY) { __builtin_amdgcn_s_sleep(DELAY); SB(); }
  for (unsigned spins = 0;; ++spins) {
    asm volatile("" ::: "memory");
    o0 = __builtin_amdgcn_raw_buffer_load_b128(rs, a0, 0, AUX_SC1);
    o1 = __builtin_amdgcn_raw_buffer_load_b128(rs, a1, 0, AUX_SC1);
    if (__builtin_amdgcn_ballot_w64((tagdiff<M0, M>(o0, f) | tagdiff<M0, M>(o1, f)) != 0u) == 0ull || spins >= SPIN_CAP) return;
  }
}
#define ASTAMP(i) do { } while (0)
#define STAMP(i) do { } while (0)
#define STAMP_FLUSH() do { } while (0)
__device__ __forceinline__ __amdgpu_buffer_rsrc_t mkrs(const void* p, int bytes) { return __builtin_amdgcn_make_buffer_rsrc((void*)p, 0, bytes, 0x00020000); }
template <int AUX> __device__ __forceinline__ v4u bld(__amdgpu_buffer_rsrc_t r, int voff, int soff) { return __builtin_amdgcn_raw_buffer_load_b128(r, voff, soff, AUX); }

__global__ void __launch_bounds__(NTHR, 3)
fused_decode(const unsigned char* __restrict__ xA, const unsigned char* __restrict__ kA, const unsigned char* __restrict__ vA,
             const unsigned char* __restrict__ wqA, const unsigned char* __restrict__ woA,
             const unsigned char* __restrict__ xB, const unsigned char* __restrict__ kB, const unsigned char* __restrict__ vB,
             const unsigned char* __restrict__ wqB, const unsigned char* __restrict__ woB,
             unsigned char* __restrict__ out, unsigned char* ws_) {
  asm volatile("" :: "s"(xA), "s"(kA), "s"(vA), "s"(wqA), "s"(woA), "s"(xB), "s"(kB), "s"(vB), "s"(wqB), "s"(woB), "s"(out), "s"(ws_));
  STAMP(15);
  extern __shared__ __attribute__((aligned(16))) unsigned char smem[];
  LAS unsigned char* L = (LAS unsigned char*)smem;
  GAS unsigned char* ws = (GAS unsigned char*)ws_;
  const int tid = threadIdx.x, l = tid & 63;
  const int wv = __builtin_amdgcn_readfirstlane(tid >> 6);
  const int bid = blockIdx.x, g = bid & 7, j = bid >> 3, b = j >> 4, s = j & 15;
  const int grp = b * 8 + g, me = grp * 16 + s;
  const __amdgpu_buffer_rsrc_t rs = mkrs(ws_, WS_END);
  if (X_EXIT >= 100) { const u64 t0_ = __builtin_amdgcn_s_memrealtime(); while (__builtin_amdgcn_s_memrealtime() - t0_ < (u64)(X_EXIT)) __builtin_amdgcn_s_sleep(1); return; }

  if (__builtin_expect(wv < NPW, 1)) {
    __builtin_amdgcn_s_setprio(3);
    const int p = wv;
    const __amdgpu_buffer_rsrc_t rx = mkrs(b ? xB : xA, 1024);
    const __amdgpu_buffer_rsrc_t rq = mkrs(b ? wqB : wqA, 512 * 8192);
    const v4u xv = bld<0>(rx, (l >> 4) * 16, (s & 3) * 256 + p * 64);
    SB();
    v4u wq[8];
    {
      const int vo = (l >> 4) * 8 * 8192 + (l & 15) * 16, so = ((s & 3) * 128 + p * 32) * 8192 + (g * 512 + (s >> 2) * 128) * 2;
#pragma unroll
      for (int i = 0; i < 8; ++i) wq[i] = bld<AUX_NT>(rq, vo, so + i * 8192);
    }
    SB();
    STAMP(16);
    if (tid == 0) *(LAS v4u*)(L + L_CNT) = v4u{0u, 0u, 0u, 0u};
    const u64 epv = __hip_atomic_load((GAS u64*)(ws + WS_EPOCH + b * 256), __ATOMIC_RELAXED, __HIP_MEMORY_SCOPE_AGENT);
    SB();
    if (KDELAY == 0) { LDSBAR(); SB(); }
    f32x2 a2[4];
#pragma unroll
    for (int i = 0; i < 8; ++i) {
      const float xs = (i & 1) ? bfhi(xv[i >> 1]) : bflo(xv[i >> 1]);
      const f32x2 x2 = {xs, xs};
#pragma unroll
      for (int c = 0; c < 4; ++c) {
        const f32x2 w2 = {bflo(wq[i][c]), bfhi(wq[i][c])};
        a2[c] = i == 0 ? x2 * w2 : __builtin_elementwise_fma(x2, w2, a2[c]);
      }
      if (KDELAY == i + 1) { asm volatile("" :: "v"(a2[0]), "v"(a2[3])); SB(); LDSBAR(); SB(); }
    }
    STAMP(1);
    {
      const f32x2 r = {tr16(tr32(a2[0][0], a2[2][0]), tr32(a2[1][0], a2[3][0])), tr16(tr32(a2[0][1], a2[2][1]), tr32(a2[1][1], a2[3][1]))};
      *(LAS f32x2*)(L + L_QP + p * 512 + (l & 15) * 32 + (l >> 5) * 16 + ((l >> 4) & 1) * 8) = r;
    }
    const u64 ep1 = (((u64)(unsigned)__builtin_amdgcn_readfirstlane((int)(epv >> 32)) << 32) | (unsigned)__builtin_amdgcn_readfirstlane((int)epv)) + 1ull;
    const u64 T = ep1 * 0x9E3779B97F4A7C15ull;
    const unsigned hi = (unsigned)(T >> 32), lo = (unsigned)T, tag = hi;
    const v4u fo = {(lo & 63u) << 4, (lo >> 6) & 1023u, (lo >> 16) & 1023u, ((lo >> 26) | (hi << 6)) & 1023u};
    asm volatile("s_waitcnt lgkmcnt(0)" :: "s"(hi), "s"(lo) : "memory");
    unsigned arr = 0;
    if (l == 0) arr = __hip_atomic_fetch_add((LAS unsigned*)(L + L_CNT), 1u, __ATOMIC_RELAXED, __HIP_MEMORY_SCOPE_WORKGROUP);
    arr = __builtin_amdgcn_readfirstlane(arr);
    if (arr == NPW - 1) {
      asm volatile("" ::: "memory");
      f32x2 q = *(volatile LAS f32x2*)(L + L_QP + l * 8);
#pragma unroll
      for (int ww = 1; ww < NPW; ++ww) q += *(volatile LAS f32x2*)(L + L_QP + ww * 512 + l * 8);
      __hip_atomic_store((GAS u64*)(ws + WS_QG) + me * 64 + l, ((u64)tag << 32) | cvtpk(q[0], q[1]), __ATOMIC_RELAXED, __HIP_MEMORY_SCOPE_AGENT);
    }
    STAMP(3);
    if (p == 3 && l < 2) *(LAS v4u*)(L + L_TAG + l * 16) = l == 0 ? fo : v4u{(hi >> 4) & 511u, (hi >> 13) & 511u, (hi >> 22) & 511u, ((hi >> 31) | (lo << 1)) & 511u};
    const bool lead = g == 0 && s == 1;
    int eo = OOB; unsigned e0, e1;
    {
      const int qo = WS_QG + (grp * 16 + 4 * p) * 512 + l * 16;
      v4u qa0, qa1, qb0, qb1, r0, r1;
      auto sig = [&](unsigned m) {
        __builtin_amdgcn_s_setprio(0);
        for (unsigned spins = 0; !(__hip_atomic_load((LAS unsigned*)(L + L_SIG), __ATOMIC_RELAXED, __HIP_MEMORY_SCOPE_WORKGROUP) & m) && spins < (SPIN_CAP << 4); ++spins) __builtin_amdgcn_s_sleep(2);
        __builtin_amdgcn_s_setprio(3);
      };
      auto tagged = [&](const v4u& a, const v4u& c) -> bool {
        return __builtin_amdgcn_ballot_w64(((a[1] ^ tag) | (a[3] ^ tag) | (c[1] ^ tag) | (c[3] ^ tag)) != 0u) == 0ull;
      };
      sig(1u);
      STAMP(7);
      asm volatile("" ::: "memory");
      qa0 = __builtin_amdgcn_raw_buffer_load_b128(rs, qo, 0, AUX_SC1);
      qa1 = __builtin_amdgcn_raw_buffer_load_b128(rs, qo + 1024, 0, AUX_SC1);
      SB();
      sig(2u);
      asm volatile("" ::: "memory");
      qb0 = __builtin_amdgcn_raw_buffer_load_b128(rs, qo, 0, AUX_SC1);
      qb1 = __builtin_amdgcn_raw_buffer_load_b128(rs, qo + 1024, 0, AUX_SC1);
      if (p == 3 && lead) eo = WS_QG + (b * 128 + l) * 512 + 4;
      e0 = __builtin_amdgcn_raw_buffer_load_b32(rs, eo, 0, AUX_SC1);
      e1 = __builtin_amdgcn_raw_buffer_load_b32(rs, eo, 64 * 512, AUX_SC1);
      SB();
      bool done = tagged(qa0, qa1);
      r0 = qa0; r1 = qa1;
      STAMP(9);
      if (!done) {
        done = tagged(qb0, qb1);
        r0 = qb0; r1 = qb1;
        STAMP(10);
        for (unsigned spins = 0; !done && spins < SPIN_CAP; ++spins) {
          asm volatile("" ::: "memory");
          r0 = __builtin_amdgcn_raw_buffer_load_b128(rs, qo, 0, AUX_SC1);
          r1 = __builtin_amdgcn_raw_buffer_load_b128(rs, qo + 1024, 0, AUX_SC1);
          done = tagged(r0, r1);
        }
      }
      const float a = tr32(bflo(r0[0]) + bflo(r1[0]), bflo(r0[2]) + bflo(r1[2])), c = tr32(bfhi(r0[0]) + bfhi(r1[0]), bfhi(r0[2]) + bfhi(r1[2]));
      *(LAS unsigned*)(L + L_QS + p * 256 + (l & 31) * 8 + (l >> 5) * 4) = cvtpk(a, c);
      STAMP(4);
    }
    LDSBAR();
    if (p == 3 && lead) {
      for (unsigned spins = 0; !__all(e0 == tag && e1 == tag) && spins < SPIN_CAP; ++spins) {
        asm volatile("" ::: "memory");
        e0 = __builtin_amdgcn_raw_buffer_load_b32(rs, eo, 0, AUX_SC1);
        e1 = __builtin_amdgcn_raw_buffer_load_b32(rs, eo, 64 * 512, AUX_SC1);
      }
      if (__all(e0 == tag && e1 == tag) && l == 0) __hip_atomic_store((GAS u64*)(ws + WS_EPOCH + b * 256), ep1, __ATOMIC_RELAXED, __HIP_MEMORY_SCOPE_AGENT);
      if (X_EXIT != 2 && X_EXIT != 6) { LDSBAR(); if (X_EXIT != 4) LDSBAR(); }
    } else if (p == 2 && X_EXIT != 2 && X_EXIT != 6) {
      LDSBAR();
      const int a0 = WS_OR + (grp * 16 + (l >> 3)) * 2048 + s * 128 + (l & 7) * 16;
      v4u o0, o1;
      if (AACK) for (unsigned spins = 0; __hip_atomic_load((LAS unsigned*)(L + L_PUB), __ATOMIC_RELAXED, __HIP_MEMORY_SCOPE_WORKGROUP) != 2u && spins < (SPIN_CAP << 6); ++spins) __builtin_amdgcn_s_sleep(1);
      poll2<ADELAY, 0x3F0u, 0x3FFu>(rs, a0, a0 + 8 * 2048, fo, o0, o1);
      STAMP(9);
      unsigned ea = (o0[0] & 15u) << ((l & 7) * 4), eb = (o1[0] & 15u) << ((l & 7) * 4);
      ea |= dppu<XOR1>(ea); eb |= dppu<XOR1>(eb);
      ea |= dppu<XOR2>(ea); eb |= dppu<XOR2>(eb);
      ea |= dppu<XOR7>(ea); eb |= dppu<XOR7>(eb);
      const float ma = __uint_as_float(ea), mb = __uint_as_float(eb);
      float Mg = fmx(ma, mb);
      Mg = x16_32_max(fmx(Mg, dpp<ROR8>(Mg)));
      const float wa = __builtin_amdgcn_exp2f(ma - Mg), wb = __builtin_amdgcn_exp2f(mb - Mg);
      float ov[4];
#pragma unroll
      for (int i = 0; i < 4; ++i) ov[i] = fmaf(wa, __uint_as_float(o0[i] & ~0x3FFu), wb * __uint_as_float(o1[i] & ~0x3FFu));
      float Lg = wa + wb;
      Lg += dpp<ROR8>(Lg);
      Lg = tr16(Lg, Lg); Lg = tr32(Lg, Lg);
      const float o = tr_reduce4(ov[0], ov[1], ov[2], ov[3]) * __builtin_amdgcn_rcpf(Lg);
      if (!(l & 8)) ((LAS float*)(L + L_OV))[(l & 7) * 4 + (l >> 5) * 2 + ((l >> 4) & 1)] = o;
      LDSBAR();
    }
    STAMP_FLUSH();
    return;
  }

  const int w = wv - NPW;
  const __amdgpu_buffer_rsrc_t rk = mkrs(b ? kB : kA, HKV * SK * 256);
  const __amdgpu_buffer_rsrc_t rv = mkrs(b ? vB : vA, HKV * SK * 256);
  const __amdgpu_buffer_rsrc_t ro = mkrs(b ? woB : woA, 4096 * 1024);
  const int kvbase = (g * SK + (SK - W) + s * 256 + w * 32) * 256;
  const int rot = (w + s) & 7;
  const int kvo = ((l & 15) >> 2) * 256 + (l >> 4) * 64 + (l & 3) * 16;
  __builtin_amdgcn_s_setprio(2);
  LDSBAR();
  SB();
  v4u kf[8], vf[8];
#pragma unroll
  for (int m = 0; m < 8; ++m) kf[m] = bld<AUX_NT>(rk, kvo, kvbase + (m ^ rot) * 1024);
  SB();
  __builtin_amdgcn_s_setprio(1);
#pragma unroll
  for (int m = 0; m < 8; ++m) {
    vf[m] = bld<AUX_NT>(rv, l * 16, kvbase + (m ^ rot) * 1024);
    SB();
    if (m + 1 == POS1 || m + 1 == POS2) {
      if (l == 0 && ((m + 1 == POS1 && w == 0) || (m + 1 == POS2 && w == 4)))
        __hip_atomic_fetch_or((LAS unsigned*)(L + L_SIG), w == 0 ? 1u : 2u, __ATOMIC_RELAXED, __HIP_MEMORY_SCOPE_WORKGROUP);
      SB();
    }
  }
  ASTAMP(1);
  LDSBAR();
  ASTAMP(2);
  const v4u qf = *(LAS v4u*)(L + L_QS + kvo);
  float sc[8];
#pragma unroll
  for (int m = 0; m < 8; ++m) {
    const f32x4 d = mfma(kf[m], qf, f32x4{0.f, 0.f, 0.f, 0.f});
    sc[m] = dpp<0xFF>(d[3]) + (dpp<0xAA>(d[2]) + (dpp<0x55>(d[1]) + dpp<0x00>(d[0])));
  }
  constexpr float C2 = 0.088388347648318440f * 1.4426950408889634f;
  const float Mc = x16_32_max(fmx(fmx3(fmx3(sc[0], sc[1], sc[2]), sc[3], sc[4]), fmx3(sc[5], sc[6], sc[7]))) * C2;
  if (l < 16 && (l & 3) == 0) ((LAS float*)(L + L_MX))[(l >> 2) * 8 + w] = Mc;
  v4u pf;
  {
    float p[8];
#pragma unroll
    for (int m = 0; m < 8; ++m) p[m] = __builtin_amdgcn_exp2f(fmaf(sc[m], C2, -Mc));
    pf = v4u{cvtpk(p[0], p[1]), cvtpk(p[2], p[3]), cvtpk(p[4], p[5]), cvtpk(p[6], p[7])};
  }
  const v4u ones = {0x3F803F80u, 0x3F803F80u, 0x3F803F80u, 0x3F803F80u};
  const f32x4 lsum = mfma(pf, ones, f32x4{0.f, 0.f, 0.f, 0.f});
  if ((l & 15) == 0) ((LAS float*)(L + L_LS))[(l >> 4) * 8 + w] = lsum[0];
  asm volatile("" ::: "memory");
  if (l == 0) __hip_atomic_fetch_add((LAS unsigned*)(L + L_MLC), 1u, __ATOMIC_RELAXED, __HIP_MEMORY_SCOPE_WORKGROUP);
  ASTAMP(3);
  SB();
  v4u wo[4];
  {
    __builtin_amdgcn_s_setprio(0);
    const int vo = (l >> 3) * 4 * 1024 + (l & 7) * 16, so = (g * 512 + s * 32) * 1024 + w * 128;
#pragma unroll
    for (int i = 0; i < 4; ++i) wo[i] = bld<AUX_NT>(ro, vo + i * 1024, so);
    SB();
  }
  {
    v4u bfr[8];
#pragma unroll
    for (int u = 0; u < 4; ++u) {
#pragma unroll
      for (int d = 0; d < 8; ++d) bfr[d][u] = __builtin_amdgcn_perm(vf[2 * u + 1][d >> 1], vf[2 * u][d >> 1], (d & 1) ? 0x07060302u : 0x05040100u);
      if (u < 3) SB();
    }
    float o[8];
#pragma unroll
    for (int d = 0; d < 8; ++d) o[d] = mfma(pf, bfr[d], f32x4{0.f, 0.f, 0.f, 0.f})[0];
    LAS f32x4* om = (LAS f32x4*)(L + L_OM + w * 2048 + l * 16);
    om[0] = f32x4{o[0], o[1], o[2], o[3]};
    om[64] = f32x4{o[4], o[5], o[6], o[7]};
  }
  float ew[8]; v4u fw = {0u, 0u, 0u, 0u};
  if (w < 2) {
    for (unsigned spins = 0; __hip_atomic_load((LAS unsigned*)(L + L_MLC), __ATOMIC_RELAXED, __HIP_MEMORY_SCOPE_WORKGROUP) != 8u && spins < (SPIN_CAP << 6); ++spins) { }
    asm volatile("" ::: "memory");
    const int h = (w * 64 + l) >> 5;
    const f32x4 m0 = *(LAS f32x4*)(L + L_MX + h * 32), m1 = *(LAS f32x4*)(L + L_MX + h * 32 + 16);
    const f32x4 l0 = *(LAS f32x4*)(L + L_LS + h * 32), l1 = *(LAS f32x4*)(L + L_LS + h * 32 + 16);
    fw = *(LAS v4u*)(L + L_TAG);
    const float Mh = fmx(fmx3(fmx3(m0[0], m0[1], m0[2]), m0[3], m1[0]), fmx3(m1[1], m1[2], m1[3]));
    float Lh = 0.f;
#pragma unroll
    for (int ww = 0; ww < 8; ++ww) { ew[ww] = __builtin_amdgcn_exp2f((ww < 4 ? m0[ww & 3] : m1[ww & 3]) - Mh); Lh = fmaf(ew[ww], ww < 4 ? l0[ww & 3] : l1[ww & 3], Lh); }
    const float inv = __builtin_amdgcn_rcpf(Lh);
#pragma unroll
    for (int ww = 0; ww < 8; ++ww) { ew[ww] *= inv; asm volatile("" : "+v"(ew[ww])); }
    fw[0] |= (__float_as_uint(Mh + __builtin_amdgcn_logf(Lh)) >> ((l & 7) * 4)) & 15u;
    asm volatile("" : "+v"(fw));
  }
  if (X_EXIT == 2) { asm volatile("" :: "v"(wo[0]), "v"(wo[1]), "v"(wo[2]), "v"(wo[3])); return; }
  if (X_EXIT == 6) return;
  ASTAMP(6);
  LDSBAR();
  ASTAMP(7);
  if (w < 2) {
    const int t = w * 64 + l;
    const int lo = (t >> 5) * 16 + ((t & 31) >> 1), h2 = t & 1;
    f32x4 a = *(LAS f32x4*)(L + L_OM + h2 * 1024 + lo * 16) * ew[0];
#pragma unroll
    for (int ww = 1; ww < 8; ++ww) a += *(LAS f32x4*)(L + L_OM + ww * 2048 + h2 * 1024 + lo * 16) * ew[ww];
    v4u o4;
#pragma unroll
    for (int i = 0; i < 4; ++i) o4[i] = ((__float_as_uint(a[i]) + 0x200u) & ~0x3FFu) | fw[i];
    __builtin_amdgcn_raw_buffer_store_b128(o4, rs, WS_OR + me * 2048 + t * 16, 0, AUX_SC1);
    if (AACK) {
      asm volatile("s_waitcnt vmcnt(0)" ::: "memory");
      if (l == 0) __hip_atomic_fetch_add((LAS unsigned*)(L + L_PUB), 1u, __ATOMIC_RELAXED, __HIP_MEMORY_SCOPE_WORKGROUP);
    }
    ASTAMP(8);
  }
  if (X_EXIT == 4) return;
  f32x2 wf[4][4];
#pragma unroll
  for (int i = 0; i < 4; ++i)
#pragma unroll
    for (int c = 0; c < 4; ++c) { wf[i][c] = f32x2{bflo(wo[i][c]), bfhi(wo[i][c])}; asm volatile("" : "+v"(wf[i][c])); }
  v4u fg = *(LAS v4u*)(L + L_TAG + 16);
  unsigned gs = (l & 2) ? ((l & 1) ? fg[3] : fg[2]) : ((l & 1) ? fg[1] : fg[0]);
  asm volatile("" : "+v"(fg), "+v"(gs));
  SB();
  LDSBAR();
  ASTAMP(10);
  {
    const f32x4 ov = *(LAS f32x4*)(L + L_OV + (l >> 3) * 16);
    f32x2 a2[4];
#pragma unroll
    for (int i = 0; i < 4; ++i) {
      const f32x2 o2 = {ov[i], ov[i]};
#pragma unroll
      for (int c = 0; c < 4; ++c) a2[c] = i == 0 ? o2 * wf[i][c] : __builtin_elementwise_fma(o2, wf[i][c], a2[c]);
    }
    const float acc[8] = {a2[0][0], a2[0][1], a2[1][0], a2[1][1], a2[2][0], a2[2][1], a2[3][0], a2[3][1]};
    const float res = tr_reduce8(acc, l);
    __builtin_amdgcn_raw_buffer_store_b32(((__float_as_uint(res) + 0x100u) & ~0x1FFu) | gs,
                                          rs, WS_IN + (b * 16 + w * 2 + (l >> 5)) * 16384 + (g * 16 + s) * 128 + (l & 31) * 4, 0, AUX_SC1);
  }
  if (X_EXIT == 5) return;
  ASTAMP(11);
  if ((s & 7) == 0) {
    const int cidx = b * 16 + g * 2 + (s >> 3);
    const int ai = WS_IN + cidx * 16384 + w * 2048 + l * 16;
    v4u v0, v1;
    if (BACK) asm volatile("s_waitcnt vmcnt(0)" ::: "memory");
    poll2<BDELAY, 0x1FFu, 0x1FFu>(rs, ai, ai + 1024, fg, v0, v1);
    ASTAMP(12);
    float sv[4];
#pragma unroll
    for (int i = 0; i < 4; ++i) sv[i] = __uint_as_float(v0[i] & ~0x1FFu) + __uint_as_float(v1[i] & ~0x1FFu);
    const float r = tr_reduce4(sv[0], sv[1], sv[2], sv[3]);
    if (!(l & 8)) ((LAS float*)(L + L_FIN))[w * 32 + (l & 7) * 4 + (l >> 5) * 2 + ((l >> 4) & 1)] = r;
    LDSBAR();
    ASTAMP(13);
    if (w == 0) {
      const int pos = (l & 3) * 8 + ((l & 31) >> 2);
      float t = 0.f;
#pragma unroll
      for (int ww = 0; ww < 8; ++ww) t += ((LAS float*)(L + L_FIN))[ww * 32 + pos];
      const float t1 = dpp<XOR1>(t);
      if (l < 32 && !(l & 1)) *(GAS unsigned*)((GAS unsigned char*)out + (b * 512 + g * 64 + (s >> 3) * 4 + (l >> 2) * 8 + (l & 3)) * 2) = cvtpk(t, t1);
    }
    ASTAMP(14);
  }
  STAMP_FLUSH();
}

extern "C" void kernel_launch(void* const* d_in, const int* in_sizes, int n_in,
                              void* d_out, int out_size, void* d_ws, size_t ws_size,
                              hipStream_t stream) {
  (void)in_sizes; (void)n_in; (void)out_size;
  unsigned char* wsp = (unsigned char*)d_ws + (WSMID ? ((ws_size / 2) & ~(size_t)4095) : 0);
  (void)hipFuncSetAttribute((const void*)fused_decode, hipFuncAttributeMaxDynamicSharedMemorySize, LDS_BYTES);
  typedef const unsigned char* P;
  hipLaunchKernelGGL(fused_decode, dim3(NWG), dim3(NTHR), LDS_BYTES, stream,
                     (P)d_in[0], (P)d_in[1], (P)d_in[2], (P)d_in[3], (P)d_in[4],
                     (P)d_in[5], (P)d_in[6], (P)d_in[7], (P)d_in[8], (P)d_in[9],
                     (unsigned char*)d_out, wsp);
}
```

```cpp
#include <hip/hip_runtime.h>
#include <hip/hip_bf16.h>
#include <stdint.h>

#define GAS __attribute__((address_space(1)))
#define LAS __attribute__((address_space(3)))
typedef unsigned v4u __attribute__((ext_vector_type(4)));
typedef unsigned v2u __attribute__((ext_vector_type(2)));
typedef float f32x4 __attribute__((ext_vector_type(4)));
typedef float f32x2 __attribute__((ext_vector_type(2)));
typedef short bf16x8 __attribute__((ext_vector_type(8)));
typedef __bf16 bf16x2 __attribute__((ext_vector_type(2)));
typedef unsigned long long u64;

constexpr int KDELAY = 2;
constexpr int POS1 = 4;
constexpr int POS2 = 2;
constexpr int ADELAY = 20;

constexpr int HKV = 8, SK = 32768, W = 4096;
constexpr int NWG = 256, NPW = 4, NTHR = (NPW + 8) * 64;
constexpr unsigned SPIN_CAP = 1u << 16;
constexpr int AUX_SC1 = 16, AUX_NT = 2, OOB = 0x7FFF0000;

constexpr int WS_EPOCH = 0;
constexpr int WS_QG = 1024;
constexpr int WS_OR = WS_QG + NWG * 512;
constexpr int WS_IN = WS_OR + NWG * 2048;
constexpr int WS_END = WS_IN + 32 * 16384;

constexpr int L_QS = 1024;
constexpr int L_MX = 2048;
constexpr int L_LS = 2176;
constexpr int L_CNT = 2304, L_SIG = 2312, L_MLC = 2316;
constexpr int L_TAG = 2368;
constexpr int L_OV = 2432;
constexpr int L_FIN = 2560;
constexpr int L_OM = 4096;
constexpr int L_QP = 20480;
constexpr int LDS_BYTES = 96 * 1024;

__device__ __forceinline__ float bflo(unsigned u) { return __builtin_bit_cast(float, u << 16); }
__device__ __forceinline__ float bfhi(unsigned u) { return __builtin_bit_cast(float, u & 0xffff0000u); }
__device__ __forceinline__ unsigned cvtpk(float lo, float hi) {
  const f32x2 v = {lo, hi};
  return __builtin_bit_cast(unsigned, __builtin_convertvector(v, bf16x2));
}
template <int CTRL> __device__ __forceinline__ float dpp(float x) {
  return __builtin_bit_cast(float, __builtin_amdgcn_mov_dpp(__builtin_bit_cast(int, x), CTRL, 0xf, 0xf, true));
}
template <int CTRL> __device__ __forceinline__ unsigned dppu(unsigned x) { return (unsigned)__builtin_amdgcn_mov_dpp((int)x, CTRL, 0xf, 0xf, true); }
constexpr int XOR1 = 0xB1, XOR2 = 0x4E, XOR7 = 0x141, ROR8 = 0x128;
__device__ __forceinline__ float fmx(float a, float b) { return __builtin_fmaxf(__builtin_fmaxf(a, b), -__builtin_inff()); }
__device__ __forceinline__ float fmx3(float a, float b, float c) { return __builtin_fmaxf(__builtin_fmaxf(a, b), c); }
__device__ __forceinline__ float tr32(float a, float b) { auto t = __builtin_amdgcn_permlane32_swap(__float_as_uint(a), __float_as_uint(b), false, false); return __uint_as_float(t[0]) + __uint_as_float(t[1]); }
__device__ __forceinline__ float tr16(float a, float b) { auto t = __builtin_amdgcn_permlane16_swap(__float_as_uint(a), __float_as_uint(b), false, false); return __uint_as_float(t[0]) + __uint_as_float(t[1]); }
__device__ __forceinline__ float x16_32_max(float x) {
  auto s = __builtin_amdgcn_permlane16_swap(__float_as_uint(x), __float_as_uint(x), false, false);
  x = fmx(__uint_as_float(s[0]), __uint_as_float(s[1]));
  auto t = __builtin_amdgcn_permlane32_swap(__float_as_uint(x), __float_as_uint(x), false, false);
  return fmx(__uint_as_float(t[0]), __uint_as_float(t[1]));
}
__device__ __forceinline__ float tr_reduce8(const float (&a)[8], int l) {
  const float r0 = tr16(tr32(a[0], a[4]), tr32(a[2], a[6])), r1 = tr16(tr32(a[1], a[5]), tr32(a[3], a[7]));
  const float u0 = r0 + dpp<ROR8>(r0), u1 = r1 + dpp<ROR8>(r1);
  return (l & 8) ? u1 : u0;
}
__device__ __forceinline__ float tr_reduce4(float a0, float a1, float a2, float a3) {
  const float r = tr16(tr32(a0, a2), tr32(a1, a3));
  return r + dpp<ROR8>(r);
}
__device__ __forceinline__ f32x4 mfma(v4u a, v4u b, f32x4 c) {
  return __builtin_amdgcn_mfma_f32_16x16x32_bf16(__builtin_bit_cast(bf16x8, a), __builtin_bit_cast(bf16x8, b), c, 0, 0, 0);
}
template <unsigned M0, unsigned M> __device__ __forceinline__ unsigned tagdiff(const v4u& a, const v4u& f) {
  return (((a[0] ^ f[0]) & M0) | ((a[1] ^ f[1]) & M)) | (((a[2] ^ f[2]) & M) | ((a[3] ^ f[3]) & M));
}
#define LDSBAR() do { asm volatile("s_waitcnt lgkmcnt(0)" ::: "memory"); __builtin_amdgcn_s_barrier(); asm volatile("" ::: "memory"); } while (0)
#define SB() __builtin_amdgcn_sched_barrier(0)
template <int DELAY, unsigned M0, unsigned M>
__device__ __forceinline__ void poll2(__amdgpu_buffer_rsrc_t rs, int a0, int a1, const v4u& f, v4u& o0, v4u& o1) {
  if (DELAY) { __builtin_amdgcn_s_sleep(DELAY); SB(); }
  for (unsigned spins = 0;; ++spins) {
    asm volatile("" ::: "memory");
    o0 = __builtin_amdgcn_raw_buffer_load_b128(rs, a0, 0, AUX_SC1);
    o1 = __builtin_amdgcn_raw_buffer_load_b128(rs, a1, 0, AUX_SC1);
    if (__builtin_amdgcn_ballot_w64((tagdiff<M0, M>(o0, f) | tagdiff<M0, M>(o1, f)) != 0u) == 0ull || spins >= SPIN_CAP) return;
  }
}
__device__ __forceinline__ __amdgpu_buffer_rsrc_t mkrs(const void* p, int bytes) { return __builtin_amdgcn_make_buffer_rsrc((void*)p, 0, bytes, 0x00020000); }
template <int AUX> __device__ __forceinline__ v4u bld(__amdgpu_buffer_rsrc_t r, int voff, int soff) { return __builtin_amdgcn_raw_buffer_load_b128(r, voff, soff, AUX); }

__global__ void __launch_bounds__(NTHR, 3)
fused_decode(const unsigned char* __restrict__ xA, const unsigned char* __restrict__ kA, const unsigned char* __restrict__ vA,
             const unsigned char* __restrict__ wqA, const unsigned char* __restrict__ woA,
             const unsigned char* __restrict__ xB, const unsigned char* __restrict__ kB, const unsigned char* __restrict__ vB,
             const unsigned char* __restrict__ wqB, const unsigned char* __restrict__ woB,
             unsigned char* __restrict__ out, unsigned char* ws_) {
  asm volatile("" :: "s"(xA), "s"(kA), "s"(vA), "s"(wqA), "s"(woA), "s"(xB), "s"(kB), "s"(vB), "s"(wqB), "s"(woB), "s"(out), "s"(ws_));
  extern __shared__ __attribute__((aligned(16))) unsigned char smem[];
  LAS unsigned char* L = (LAS unsigned char*)smem;
  GAS unsigned char* ws = (GAS unsigned char*)ws_;
  const int tid = threadIdx.x, l = tid & 63;
  const int wv = __builtin_amdgcn_readfirstlane(tid >> 6);
  const int bid = blockIdx.x, g = bid & 7, j = bid >> 3, b = j >> 4, s = j & 15;
  const int grp = b * 8 + g, me = grp * 16 + s;
  const __amdgpu_buffer_rsrc_t rs = mkrs(ws_, WS_END);

  if (__builtin_expect(wv < NPW, 1)) {
    __builtin_amdgcn_s_setprio(3);
    const int p = wv;
    const __amdgpu_buffer_rsrc_t rx = mkrs(b ? xB : xA, 1024);
    const __amdgpu_buffer_rsrc_t rq = mkrs(b ? wqB : wqA, 512 * 8192);
    const v4u xv = bld<0>(rx, (l >> 4) * 16, (s & 3) * 256 + p * 64);
    SB();
    v4u wq[8];
    {
      const int vo = (l >> 4) * 8 * 8192 + (l & 15) * 16, so = ((s & 3) * 128 + p * 32) * 8192 + (g * 512 + (s >> 2) * 128) * 2;
#pragma unroll
      for (int i = 0; i < 8; ++i) wq[i] = bld<AUX_NT>(rq, vo, so + i * 8192);
    }
    SB();
    if (tid == 0) *(LAS v4u*)(L + L_CNT) = v4u{0u, 0u, 0u, 0u};
    const u64 epv = __hip_atomic_load((GAS u64*)(ws + WS_EPOCH + b * 256), __ATOMIC_RELAXED, __HIP_MEMORY_SCOPE_AGENT);
    SB();
    __builtin_amdgcn_raw_buffer_store_b128(v4u{0u, 0u, 0u, 0u}, rs,
        tid < 128 ? WS_OR + me * 2048 + tid * 16 : WS_IN + (b * 16 + ((tid - 128) >> 3)) * 16384 + (g * 16 + s) * 128 + (tid & 7) * 16, 0, AUX_SC1);
    SB();
    f32x2 a2[4];
#pragma unroll
    for (int i = 0; i < 8; ++i) {
      const float xs = (i & 1) ? bfhi(xv[i >> 1]) : bflo(xv[i >> 1]);
      const f32x2 x2 = {xs, xs};
#pragma unroll
      for (int c = 0; c < 4; ++c) {
        const f32x2 w2 = {bflo(wq[i][c]), bfhi(wq[i][c])};
        a2[c] = i == 0 ? x2 * w2 : __builtin_elementwise_fma(x2, w2, a2[c]);
      }
      if (KDELAY == i + 1) { asm volatile("" :: "v"(a2[0]), "v"(a2[3])); SB(); LDSBAR(); SB(); }
    }
    {
      const f32x2 r = {tr16(tr32(a2[0][0], a2[2][0]), tr32(a2[1][0], a2[3][0])), tr16(tr32(a2[0][1], a2[2][1]), tr32(a2[1][1], a2[3][1]))};
      *(LAS f32x2*)(L + L_QP + p * 512 + (l & 15) * 32 + (l >> 5) * 16 + ((l >> 4) & 1) * 8) = r;
    }
    const u64 ep1 = (((u64)(unsigned)__builtin_amdgcn_readfirstlane((int)(epv >> 32)) << 32) | (unsigned)__builtin_amdgcn_readfirstlane((int)epv)) + 1ull;
    const u64 T = ep1 * 0x9E3779B97F4A7C15ull + 0x632BE59BD9B4E019ull;
    const unsigned hi = (unsigned)(T >> 32), lo = (unsigned)T, tag = hi;
    const v4u fo = {(lo & 63u) << 4, (lo >> 6) & 1023u, (lo >> 16) & 1023u, ((lo >> 26) | (hi << 6)) & 1023u};
    asm volatile("s_waitcnt lgkmcnt(0)" :: "s"(hi), "s"(lo) : "memory");
    unsigned arr = 0;
    if (l == 0) arr = __hip_atomic_fetch_add((LAS unsigned*)(L + L_CNT), 1u, __ATOMIC_RELAXED, __HIP_MEMORY_SCOPE_WORKGROUP);
    arr = __builtin_amdgcn_readfirstlane(arr);
    if (arr == NPW - 1) {
      asm volatile("" ::: "memory");
      f32x2 q = *(volatile LAS f32x2*)(L + L_QP + l * 8);
#pragma unroll
      for (int ww = 1; ww < NPW; ++ww) q += *(volatile LAS f32x2*)(L + L_QP + ww * 512 + l * 8);
      __hip_atomic_store((GAS u64*)(ws + WS_QG) + me * 64 + l, ((u64)tag << 32) | cvtpk(q[0], q[1]), __ATOMIC_RELAXED, __HIP_MEMORY_SCOPE_AGENT);
    }
    if (p == 3 && l < 2) *(LAS v4u*)(L + L_TAG + l * 16) = l == 0 ? fo : v4u{(hi >> 4) & 511u, (hi >> 13) & 511u, (hi >> 22) & 511u, ((hi >> 31) | (lo << 1)) & 511u};
    const bool lead = g == 0 && s == 1;
    int eo = OOB; unsigned e0, e1;
    {
      const int qo = WS_QG + (grp * 16 + 4 * p) * 512 + l * 16;
      v4u qa0, qa1, qb0, qb1, r0, r1;
      auto sig = [&](unsigned m) {
        __builtin_amdgcn_s_setprio(0);
        for (unsigned spins = 0; !(__hip_atomic_load((LAS unsigned*)(L + L_SIG), __ATOMIC_RELAXED, __HIP_MEMORY_SCOPE_WORKGROUP) & m) && spins < (SPIN_CAP << 4); ++spins) __builtin_amdgcn_s_sleep(2);
        __builtin_amdgcn_s_setprio(3);
      };
      auto tagged = [&](const v4u& a, const v4u& c) -> bool {
        return __builtin_amdgcn_ballot_w64(((a[1] ^ tag) | (a[3] ^ tag) | (c[1] ^ tag) | (c[3] ^ tag)) != 0u) == 0ull;
      };
      sig(1u);
      asm volatile("" ::: "memory");
      qa0 = __builtin_amdgcn_raw_buffer_load_b128(rs, qo, 0, AUX_SC1);
      qa1 = __builtin_amdgcn_raw_buffer_load_b128(rs, qo + 1024, 0, AUX_SC1);
      SB();
      sig(2u);
      asm volatile("" ::: "memory");
      qb0 = __builtin_amdgcn_raw_buffer_load_b128(rs, qo, 0, AUX_SC1);
      qb1 = __builtin_amdgcn_raw_buffer_load_b128(rs, qo + 1024, 0, AUX_SC1);
      if (p == 3 && lead) eo = WS_QG + (b * 128 + l) * 512 + 4;
      e0 = __builtin_amdgcn_raw_buffer_load_b32(rs, eo, 0, AUX_SC1);
      e1 = __builtin_amdgcn_raw_buffer_load_b32(rs, eo, 64 * 512, AUX_SC1);
      SB();
      bool done = tagged(qa0, qa1);
      r0 = qa0; r1 = qa1;
      if (!done) {
        done = tagged(qb0, qb1);
        r0 = qb0; r1 = qb1;
        for (unsigned spins = 0; !done && spins < SPIN_CAP; ++spins) {
          asm volatile("" ::: "memory");
          r0 = __builtin_amdgcn_raw_buffer_load_b128(rs, qo, 0, AUX_SC1);
          r1 = __builtin_amdgcn_raw_buffer_load_b128(rs, qo + 1024, 0, AUX_SC1);
          done = tagged(r0, r1);
        }
      }
      const float a = tr32(bflo(r0[0]) + bflo(r1[0]), bflo(r0[2]) + bflo(r1[2])), c = tr32(bfhi(r0[0]) + bfhi(r1[0]), bfhi(r0[2]) + bfhi(r1[2]));
      *(LAS unsigned*)(L + L_QS + p * 256 + (l & 31) * 8 + (l >> 5) * 4) = cvtpk(a, c);
    }
    LDSBAR();
    if (p == 3 && lead) {
      for (unsigned spins = 0; !__all(e0 == tag && e1 == tag) && spins < SPIN_CAP; ++spins) {
        asm volatile("" ::: "memory");
        e0 = __builtin_amdgcn_raw_buffer_load_b32(rs, eo, 0, AUX_SC1);
        e1 = __builtin_amdgcn_raw_buffer_load_b32(rs, eo, 64 * 512, AUX_SC1);
      }
      if (__all(e0 == tag && e1 == tag) && l == 0) __hip_atomic_store((GAS u64*)(ws + WS_EPOCH + b * 256), ep1, __ATOMIC_RELAXED, __HIP_MEMORY_SCOPE_AGENT);
      LDSBAR(); LDSBAR();
    } else if (p == 2) {
      LDSBAR();
      const int a0 = WS_OR + (grp * 16 + (l >> 3)) * 2048 + s * 128 + (l & 7) * 16;
      v4u o0, o1;
      poll2<ADELAY, 0x3F0u, 0x3FFu>(rs, a0, a0 + 8 * 2048, fo, o0, o1);
      unsigned ea = (o0[0] & 15u) << ((l & 7) * 4), eb = (o1[0] & 15u) << ((l & 7) * 4);
      ea |= dppu<XOR1>(ea); eb |= dppu<XOR1>(eb);
      ea |= dppu<XOR2>(ea); eb |= dppu<XOR2>(eb);
      ea |= dppu<XOR7>(ea); eb |= dppu<XOR7>(eb);
      const float ma = __uint_as_float(ea), mb = __uint_as_float(eb);
      float Mg = fmx(ma, mb);
      Mg = x16_32_max(fmx(Mg, dpp<ROR8>(Mg)));
      const float wa = __builtin_amdgcn_exp2f(ma - Mg), wb = __builtin_amdgcn_exp2f(mb - Mg);
      float ov[4];
#pragma unroll
      for (int i = 0; i < 4; ++i) ov[i] = fmaf(wa, __uint_as_float(o0[i] & ~0x3FFu), wb * __uint_as_float(o1[i] & ~0x3FFu));
      float Lg = wa + wb;
      Lg += dpp<ROR8>(Lg);
      Lg = tr16(Lg, Lg); Lg = tr32(Lg, Lg);
      const float o = tr_reduce4(ov[0], ov[1], ov[2], ov[3]) * __builtin_amdgcn_rcpf(Lg);
      if (!(l & 8)) ((LAS float*)(L + L_OV))[(l & 7) * 4 + (l >> 5) * 2 + ((l >> 4) & 1)] = o;
      LDSBAR();
    }
    return;
  }

  const int w = wv - NPW;
  const __amdgpu_buffer_rsrc_t rk = mkrs(b ? kB : kA, HKV * SK * 256);
  const __amdgpu_buffer_rsrc_t rv = mkrs(b ? vB : vA, HKV * SK * 256);
  const __amdgpu_buffer_rsrc_t ro = mkrs(b ? woB : woA, 4096 * 1024);
  const int kvbase = (g * SK + (SK - W) + s * 256 + w * 32) * 256;
  const int rot = (w + s) & 7;
  const int kvo = ((l & 15) >> 2) * 256 + (l >> 4) * 64 + (l & 3) * 16;
  __builtin_amdgcn_s_setprio(2);
  LDSBAR();
  SB();
  v4u kf[8], vf[8];
#pragma unroll
  for (int m = 0; m < 8; ++m) kf[m] = bld<AUX_NT>(rk, kvo, kvbase + (m ^ rot) * 1024);
  SB();
  __builtin_amdgcn_s_setprio(1);
#pragma unroll
  for (int m = 0; m < 8; ++m) {
    vf[m] = bld<AUX_NT>(rv, l * 16, kvbase + (m ^ rot) * 1024);
    SB();
    if (m + 1 == POS1 || m + 1 == POS2) {
      if (l == 0 && ((m + 1 == POS1 && w == 0) || (m + 1 == POS2 && w == 4)))
        __hip_atomic_fetch_or((LAS unsigned*)(L + L_SIG), w == 0 ? 1u : 2u, __ATOMIC_RELAXED, __HIP_MEMORY_SCOPE_WORKGROUP);
      SB();
    }
  }
  LDSBAR();
  const v4u qf = *(LAS v4u*)(L + L_QS + kvo);
  float sc[8];
#pragma unroll
  for (int m = 0; m < 8; ++m) {
    const f32x4 d = mfma(kf[m], qf, f32x4{0.f, 0.f, 0.f, 0.f});
    sc[m] = dpp<0xFF>(d[3]) + (dpp<0xAA>(d[2]) + (dpp<0x55>(d[1]) + dpp<0x00>(d[0])));
  }
  constexpr float C2 = 0.088388347648318440f * 1.4426950408889634f;
  const float Mc = x16_32_max(fmx(fmx3(fmx3(sc[0], sc[1], sc[2]), sc[3], sc[4]), fmx3(sc[5], sc[6], sc[7]))) * C2;
  if (l < 16 && (l & 3) == 0) ((LAS float*)(L + L_MX))[(l >> 2) * 8 + w] = Mc;
  v4u pf;
  {
    float p[8];
#pragma unroll
    for (int m = 0; m < 8; ++m) p[m] = __builtin_amdgcn_exp2f(fmaf(sc[m], C2, -Mc));
    pf = v4u{cvtpk(p[0], p[1]), cvtpk(p[2], p[3]), cvtpk(p[4], p[5]), cvtpk(p[6], p[7])};
  }
  const v4u ones = {0x3F803F80u, 0x3F803F80u, 0x3F803F80u, 0x3F803F80u};
  const f32x4 lsum = mfma(pf, ones, f32x4{0.f, 0.f, 0.f, 0.f});
  if ((l & 15) == 0) ((LAS float*)(L + L_LS))[(l >> 4) * 8 + w] = lsum[0];
  asm volatile("" ::: "memory");
  if (l == 0) __hip_atomic_fetch_add((LAS unsigned*)(L + L_MLC), 1u, __ATOMIC_RELAXED, __HIP_MEMORY_SCOPE_WORKGROUP);
  SB();
  v4u wo[4];
  {
    __builtin_amdgcn_s_setprio(0);
    const int vo = (l >> 3) * 4 * 1024 + (l & 7) * 16, so = (g * 512 + s * 32) * 1024 + w * 128;
#pragma unroll
    for (int i = 0; i < 4; ++i) wo[i] = bld<AUX_NT>(ro, vo + i * 1024, so);
    SB();
  }
  {
    v4u bfr[8];
#pragma unroll
    for (int u = 0; u < 4; ++u) {
#pragma unroll
      for (int d = 0; d < 8; ++d) bfr[d][u] = __builtin_amdgcn_perm(vf[2 * u + 1][d >> 1], vf[2 * u][d >> 1], (d & 1) ? 0x07060302u : 0x05040100u);
      if (u < 3) SB();
    }
    float o[8];
#pragma unroll
    for (int d = 0; d < 8; ++d) o[d] = mfma(pf, bfr[d], f32x4{0.f, 0.f, 0.f, 0.f})[0];
    LAS f32x4* om = (LAS f32x4*)(L + L_OM + w * 2048 + l * 16);
    om[0] = f32x4{o[0], o[1], o[2], o[3]};
    om[64] = f32x4{o[4], o[5], o[6], o[7]};
  }
  float ew[8]; v4u fw = {0u, 0u, 0u, 0u};
  if (w < 2) {
    for (unsigned spins = 0; __hip_atomic_load((LAS unsigned*)(L + L_MLC), __ATOMIC_RELAXED, __HIP_MEMORY_SCOPE_WORKGROUP) != 8u && spins < (SPIN_CAP << 6); ++spins) { }
    asm volatile("" ::: "memory");
    const int h = (w * 64 + l) >> 5;
    const f32x4 m0 = *(LAS f32x4*)(L + L_MX + h * 32), m1 = *(LAS f32x4*)(L + L_MX + h * 32 + 16);
    const f32x4 l0 = *(LAS f32x4*)(L + L_LS + h * 32), l1 = *(LAS f32x4*)(L + L_LS + h * 32 + 16);
    fw = *(LAS v4u*)(L + L_TAG);
    const float Mh = fmx(fmx3(fmx3(m0[0], m0[1], m0[2]), m0[3], m1[0]), fmx3(m1[1], m1[2], m1[3]));
    float Lh = 0.f;
#pragma unroll
    for (int ww = 0; ww < 8; ++ww) { ew[ww] = __builtin_amdgcn_exp2f((ww < 4 ? m0[ww & 3] : m1[ww & 3]) - Mh); Lh = fmaf(ew[ww], ww < 4 ? l0[ww & 3] : l1[ww & 3], Lh); }
    const float inv = __builtin_amdgcn_rcpf(Lh);
#pragma unroll
    for (int ww = 0; ww < 8; ++ww) { ew[ww] *= inv; asm volatile("" : "+v"(ew[ww])); }
    fw[0] |= (__float_as_uint(Mh + __builtin_amdgcn_logf(Lh)) >> ((l & 7) * 4)) & 15u;
    asm volatile("" : "+v"(fw));
  }
  LDSBAR();
  if (w < 2) {
    const int t = w * 64 + l;
    const int lo = (t >> 5) * 16 + ((t & 31) >> 1), h2 = t & 1;
    f32x4 a = *(LAS f32x4*)(L + L_OM + h2 * 1024 + lo * 16) * ew[0];
#pragma unroll
    for (int ww = 1; ww < 8; ++ww) a += *(LAS f32x4*)(L + L_OM + ww * 2048 + h2 * 1024 + lo * 16) * ew[ww];
    v4u o4;
#pragma unroll
    for (int i = 0; i < 4; ++i) o4[i] = ((__float_as_uint(a[i]) + 0x200u) & ~0x3FFu) | fw[i];
    __builtin_amdgcn_raw_buffer_store_b128(o4, rs, WS_OR + me * 2048 + t * 16, 0, AUX_SC1);
  }
  f32x2 wf[4][4];
#pragma unroll
  for (int i = 0; i < 4; ++i)
#pragma unroll
    for (int c = 0; c < 4; ++c) { wf[i][c] = f32x2{bflo(wo[i][c]), bfhi(wo[i][c])}; asm volatile("" : "+v"(wf[i][c])); }
  v4u fg = *(LAS v4u*)(L + L_TAG + 16);
  unsigned gs = (l & 2) ? ((l & 1) ? fg[3] : fg[2]) : ((l & 1) ? fg[1] : fg[0]);
  asm volatile("" : "+v"(fg), "+v"(gs));
  SB();
  LDSBAR();
  {
    const f32x4 ov = *(LAS f32x4*)(L + L_OV + (l >> 3) * 16);
    f32x2 a2[4];
#pragma unroll
    for (int i = 0; i < 4; ++i) {
      const f32x2 o2 = {ov[i], ov[i]};
#pragma unroll
      for (int c = 0; c < 4; ++c) a2[c] = i == 0 ? o2 * wf[i][c] : __builtin_elementwise_fma(o2, wf[i][c], a2[c]);
    }
    const float acc[8] = {a2[0][0], a2[0][1], a2[1][0], a2[1][1], a2[2][0], a2[2][1], a2[3][0], a2[3][1]};
    const float res = tr_reduce8(acc, l);
    __builtin_amdgcn_raw_buffer_store_b32(((__float_as_uint(res) + 0x100u) & ~0x1FFu) | gs,
                                          rs, WS_IN + (b * 16 + w * 2 + (l >> 5)) * 16384 + (g * 16 + s) * 128 + (l & 31) * 4, 0, AUX_SC1);
  }
  if ((s & 7) == 0) {
    const int cidx = b * 16 + g * 2 + (s >> 3);
    const int ai = WS_IN + cidx * 16384 + w * 2048 + l * 16;
    v4u v0, v1;
    asm volatile("s_waitcnt vmcnt(0)" ::: "memory");
    poll2<0, 0x1FFu, 0x1FFu>(rs, ai, ai + 1024, fg, v0, v1);
    float sv[4];
#pragma unroll
    for (int i = 0; i < 4; ++i) sv[i] = __uint_as_float(v0[i] & ~0x1FFu) + __uint_as_float(v1[i] & ~0x1FFu);
    const float r = tr_reduce4(sv[0], sv[1], sv[2], sv[3]);
    if (!(l & 8)) ((LAS float*)(L + L_FIN))[w * 32 + (l & 7) * 4 + (l >> 5) * 2 + ((l >> 4) & 1)] = r;
    LDSBAR();
    if (w == 0) {
      const int pos = (l & 3) * 8 + ((l & 31) >> 2);
      float t = 0.f;
#pragma unroll
      for (int ww = 0; ww < 8; ++ww) t += ((LAS float*)(L + L_FIN))[ww * 32 + pos];
      const float t1 = dpp<XOR1>(t);
      if (l < 32 && !(l & 1)) *(GAS unsigned*)((GAS unsigned char*)out + (b * 512 + g * 64 + (s >> 3) * 4 + (l >> 2) * 8 + (l & 3)) * 2) = cvtpk(t, t1);
    }
  }
}

extern "C" void kernel_launch(void* const* d_in, const int* in_sizes, int n_in,
                              void* d_out, int out_size, void* d_ws, size_t ws_size,
                              hipStream_t stream) {
  (void)in_sizes; (void)n_in; (void)out_size;
  unsigned char* wsp = (unsigned char*)d_ws + ((ws_size / 2) & ~(size_t)4095);
  (void)hipFuncSetAttribute((const void*)fused_decode, hipFuncAttributeMaxDynamicSharedMemorySize, LDS_BYTES);
  typedef const unsigned char* P;
  hipLaunchKernelGGL(fused_decode, dim3(NWG), dim3(NTHR), LDS_BYTES, stream,
                     (P)d_in[0], (P)d_in[1], (P)d_in[2], (P)d_in[3], (P)d_in[4],
                     (P)d_in[5], (P)d_in[6], (P)d_in[7], (P)d_in[8], (P)d_in[9],
                     (unsigned char*)d_out, wsp);
}
```
